```python
import torch

def _softmax(x, axis=-1):
    return torch.softmax(x, dim=axis)
def _gelu(x):
    return torch.nn.functional.gelu(x, approximate="tanh")
def _silu(x):
    return torch.nn.functional.silu(x)
def _sigmoid(x):
    return torch.sigmoid(x)
def _layernorm(x, axis=-1, eps=1e-5):
    m = x.mean(dim=axis, keepdim=True)
    v = x.var(dim=axis, keepdim=True, unbiased=False)
    return (x - m) / torch.sqrt(v + eps)
def _scatter_add(tgt, idx, src, axis=0):
    return tgt.clone().index_add_(axis, idx, src)
def _segment_sum(src, seg, n):
    out = torch.zeros((n,) + src.shape[1:], dtype=src.dtype)
    return out.index_add_(0, seg, src)
def _pad(x, pw):
    flat = tuple(v for pair in reversed(pw) for v in pair)
    return torch.nn.functional.pad(x, flat)

kernel_name = 'gated_mix__linear_attn_block__mixer_token_mlp__prompt'

def setup_inputs():

    _w = (lambda a: (lambda t: t.to(torch.bfloat16) if t.is_floating_point() else t)(torch.as_tensor(a)))
    def _A_setup():
        B, S, D = 1, 2048, 512
        return {
            "x": _w((lambda *s: torch.randn(*s, dtype=torch.bfloat16) * 0.05)(B, S, D)),
            "wqkv": _w((lambda *s: torch.randn(*s, dtype=torch.bfloat16) * 0.4)(D, 3 * D)),
            "wo": _w((lambda *s: torch.randn(*s, dtype=torch.bfloat16) * 0.05)(D, D)),
        }
    _A_in = _A_setup()

    def _B_setup():
        B, S, D, F = 1, 2048, 512, 1376
        return {
            "x": _w((lambda *s: torch.randn(*s, dtype=torch.bfloat16) * 0.05)(B, S, D)),
            "w1": _w((lambda *s: torch.randn(*s, dtype=torch.bfloat16) * 0.4)(S, F) * (S ** -0.5)),
            "b1": _w((lambda *s: torch.randn(*s, dtype=torch.bfloat16) * 3.2)(F) * 0.02),
            "w2": _w((lambda *s: torch.randn(*s, dtype=torch.bfloat16) * 0.05)(F, S) * (F ** -0.5)),
            "b2": _w((lambda *s: torch.randn(*s, dtype=torch.bfloat16) * 0.1)(S) * 0.02),
        }
    _B_in = _B_setup()

    return {
        "A_x": _A_in["x"],
        "A_wqkv": _A_in["wqkv"],
        "A_wo": _A_in["wo"],
        "B_x": _B_in["x"],
        "B_w1": _B_in["w1"],
        "B_b1": _B_in["b1"],
        "B_w2": _B_in["w2"],
        "B_b2": _B_in["b2"],
        "gate_w": _w((lambda *s: torch.randn(*s, dtype=torch.bfloat16) * 0.05)(512) * 16.0),
    }

def reference(A_x, A_wqkv, A_wo, B_x, B_w1, B_b1, B_w2, B_b2, gate_w):
    def _A_ref(x, wqkv, wo):
        B, S, D = x.shape
        H = 32; Dh = D // H
        qkv = (lambda x, *d: x.permute(*d))((x @ wqkv).reshape(B, S, 3, H, Dh), 2, 0, 3, 1, 4)
        q, k, v = qkv[0], qkv[1], qkv[2]
        phi = lambda t: torch.where(t > 0, t + 1, torch.exp(t)) + 1e-6
        pq, pk = phi(q), phi(k)
        kv = torch.einsum("bhsd,bhse->bhde", pk, v)
        z = torch.einsum("bhsd->bhd", pk)
        num = torch.einsum("bhqd,bhde->bhqe", pq, kv)
        den = torch.einsum("bhqd,bhd->bhq", pq, z)[..., None] + 1e-6
        o = num / den
        return (lambda x, *d: x.permute(*d))(o, 0, 2, 1, 3).reshape(B, S, D) @ wo

    def _B_ref(x, w1, b1, w2, b2):
        B, S, D = x.shape
        h = _layernorm(x, axis=-1)
        u = torch.einsum("bsd,sf->bdf", h, w1) + b1
        u = _gelu(u)
        y = torch.einsum("bdf,fs->bds", u, w2) + b2
        return (lambda x, *d: x.permute(*d))(y, 0, 2, 1)

    x = A_x
    g = _sigmoid((x * gate_w).sum(-1, keepdims=True))
    a = _A_ref(x, A_wqkv, A_wo)
    b = _B_ref(x, B_w1, B_b1, B_w2, B_b2)
    return g * a + (1 - g) * b
```

```cpp
#include <hip/hip_runtime.h>
#include <hip/hip_bf16.h>
#include <stdint.h>

typedef unsigned short u16;
__device__ __forceinline__ float bf2f(u16 v){ return __uint_as_float(((unsigned)v)<<16); }
__device__ __forceinline__ u16 f2bf(float f){ __hip_bfloat16 b = __float2bfloat16(f); return *(u16*)&b; }

constexpr int S=2048, D=512, F=1376, H=32, DH=16;

__global__ void k_stats(const u16* x, const u16* gw, float* mean, float* rstd, float* g){
  int s = blockIdx.x; int t = threadIdx.x;
  float sum=0, gs=0; float v[8];
  for(int i=0;i<8;i++){ v[i]=bf2f(x[s*D+t*8+i]); sum+=v[i]; gs+=v[i]*bf2f(gw[t*8+i]); }
  for(int o=32;o>0;o>>=1){ sum+=__shfl_xor(sum,o); gs+=__shfl_xor(gs,o);}
  float m=sum/D; float q=0;
  for(int i=0;i<8;i++){ float d=v[i]-m; q+=d*d; }
  for(int o=32;o>0;o>>=1) q+=__shfl_xor(q,o);
  if(t==0){ mean[s]=m; rstd[s]=rsqrtf(q/D+1e-5f); g[s]=1.f/(1.f+expf(-gs)); }
}
__global__ void k_qkv(const u16* x, const u16* w, float* qkv){
  int n = blockIdx.x*256+threadIdx.x; int s = blockIdx.y;
  float a=0; for(int k=0;k<D;k++) a+=bf2f(x[s*D+k])*bf2f(w[k*3*D+n]);
  if(n<2*D) a = (a>0? a+1.f: expf(a))+1e-6f;
  qkv[s*3*D+n]=a;
}
__global__ void k_kv(const float* qkv, float* kv, float* z){
  int h=blockIdx.x; int d=threadIdx.x/16, e=threadIdx.x%16;
  float a=0, zz=0; for(int s=0;s<S;s++){ float pk=qkv[s*3*D+D+h*16+d]; a+=pk*qkv[s*3*D+2*D+h*16+e]; zz+=pk; }
  kv[h*256+d*16+e]=a; if(e==0) z[h*16+d]=zz;
}
__global__ void k_o(const float* qkv, const float* kv, const float* z, float* o){
  int s=blockIdx.x; int n=threadIdx.x; int h=n/16, e=n%16;
  float num=0, den=0; for(int d=0;d<16;d++){ float pq=qkv[s*3*D+h*16+d]; num+=pq*kv[h*256+d*16+e]; den+=pq*z[h*16+d]; }
  o[s*D+n]=num/(den+1e-6f);
}
__global__ void k_u(const u16* x, const float* mean, const float* rstd, const u16* w1, const u16* b1, float* u){
  int f=blockIdx.x*32+threadIdx.x%32; int d=blockIdx.y*8+threadIdx.x/32;
  float a=0; for(int s=0;s<S;s++) a+=(bf2f(x[s*D+d])-mean[s])*rstd[s]*bf2f(w1[s*F+f]);
  a+=bf2f(b1[f]);
  float t=0.7978845608028654f*(a+0.044715f*a*a*a);
  u[d*F+f]=0.5f*a*(1.f+tanhf(t));
}
__global__ void k_out(const float* o, const u16* wo, const float* u, const u16* w2, const u16* b2, const float* g, u16* out){
  int s=blockIdx.x; int n=threadIdx.x;
  float a=0; for(int k=0;k<D;k++) a+=o[s*D+k]*bf2f(wo[k*D+n]);
  float y=0; for(int f=0;f<F;f++) y+=u[n*F+f]*bf2f(w2[f*S+s]);
  y+=bf2f(b2[s]);
  float gg=g[s];
  out[s*D+n]=f2bf(gg*a+(1.f-gg)*y);
}

extern "C" void kernel_launch(void* const* d_in, const int* in_sizes, int n_in,
                              void* d_out, int out_size, void* d_ws, size_t ws_size,
                              hipStream_t stream) {
  const u16* x=(const u16*)d_in[0]; const u16* wqkv=(const u16*)d_in[1]; const u16* wo=(const u16*)d_in[2];
  const u16* w1=(const u16*)d_in[4]; const u16* b1=(const u16*)d_in[5]; const u16* w2=(const u16*)d_in[6];
  const u16* b2=(const u16*)d_in[7]; const u16* gw=(const u16*)d_in[8];
  float* ws=(float*)d_ws;
  float* mean=ws; float* rstd=mean+S; float* g=rstd+S; float* qkv=g+S; float* kv=qkv+S*3*D; float* z=kv+H*256; float* o=z+D; float* u=o+S*D;
  k_stats<<<S,64,0,stream>>>(x,gw,mean,rstd,g);
  k_qkv<<<dim3(6,S),256,0,stream>>>(x,wqkv,qkv);
  k_kv<<<H,256,0,stream>>>(qkv,kv,z);
  k_o<<<S,512,0,stream>>>(qkv,kv,z,o);
  k_u<<<dim3(F/32,D/8),256,0,stream>>>(x,mean,rstd,w1,b1,u);
  k_out<<<S,512,0,stream>>>(o,wo,u,w2,b2,g,(u16*)d_out);
}
```

```cpp
#include <hip/hip_runtime.h>
#include <stdint.h>

struct Srd { const void* p; uint32_t bytes; uint32_t w3; };
struct Args { Srd b[10]; uint32_t map[512]; };

__global__ __launch_bounds__(256, 1) void fused_kernel(Args args) {
  const unsigned tid = threadIdx.x, wg = blockIdx.x;
  auto ka = __builtin_amdgcn_kernarg_segment_ptr();
  asm volatile(R"ASM(
s_mov_b64 s[0:1], %0
s_mov_b32 s2, %1
v_mov_b32 v0, %2
s_load_dwordx16 s[4:19], s[0:1], 0x0
s_lshl_b32 s54, s2, 3
s_load_dwordx2 s[44:45], s[0:1], s54 offset:0xa0
s_load_dwordx16 s[20:35], s[0:1], 0x40
s_load_dwordx8 s[36:43], s[0:1], 0x80
s_getpc_b64 s[60:61]
PC0:
v_lshrrev_b32 v1, 6, v0
s_nop 0
v_readfirstlane_b32 s69, v1
s_cmp_lg_u32 s69, 3
s_cbranch_scc1 CP_1
s_lshr_b32 s51, s2, 3
v_and_b32 v1, 15, v0
v_lshl_add_u32 v1, v1, 5, s51
v_lshlrev_b32 v1, 7, v1
v_min_u32 v1, PCEND-PC0-4, v1
s_mov_b64 exec, 0xffff
global_load_dword v118, v1, s[60:61]
s_mov_b64 exec, -1
CP_1:
s_and_b32 s51, s2, 6
s_cmp_eq_u32 s51, 6
s_cselect_b32 s51, 128, 64
s_cmp_lt_u32 s2, s51
s_cbranch_scc0 ROLE_U
v_and_b32 v99, 31, v0
v_lshrrev_b32 v98, 5, v0
v_lshlrev_b32 v99, 4, v99
v_lshl_add_u32 v2, v98, 10, v99
v_and_b32 v240, 63, v0
v_lshlrev_b32 v241, 4, v240
v_lshrrev_b32 v1, 2, v0
v_and_b32 v1, 4, v1
v_and_b32 v3, 7, v0
v_xor_b32 v1, v1, v3
v_lshlrev_b32 v243, 4, v1
v_bfe_u32 v1, v0, 2, 2
v_lshlrev_b32 v242, 7, v1
v_bfe_u32 v1, v0, 3, 1
v_lshl_or_b32 v242, v1, 6, v242
v_bfe_u32 v1, v0, 4, 1
v_lshl_or_b32 v242, v1, 5, v242
v_and_b32 v1, 3, v0
v_lshl_or_b32 v242, v1, 3, v242
s_mul_i32 s48, s69, 24576
s_and_b32 s51, s69, 1
s_lshl_b32 s51, s51, 2
v_lshrrev_b32 v1, 4, v240
v_and_b32 v3, 7, v240
v_xor_b32 v4, v1, v3
v_xor_b32 v4, s51, v4
v_lshrrev_b32 v5, 3, v240
v_lshlrev_b32 v6, 10, v5
v_lshl_add_u32 v217, v4, 4, v6
v_xor_b32 v7, 64, v217
s_mul_i32 s65, s69, 7168
s_cmp_eq_u32 s69, 3
s_cselect_b32 s52, 0x40000000, 0
v_add_u32 v218, 7168, v7
v_add_u32 v219, 14336, v217
v_add_u32 v220, 21504, v7
v_add_u32 v221, 32768, v217
v_add_u32 v222, 39936, v7
v_add_u32 v222, s52, v222
v_add_u32 v223, 47104, v217
v_add_u32 v223, s52, v223
v_and_b32 v1, 1, v1
v_lshlrev_b32 v1, 1, v1
v_xor_b32 v4, v1, v3
v_mul_u32_u24 v6, 3072, v5
v_lshl_add_u32 v224, v4, 4, v6
v_xor_b32 v7, 64, v224
v_add_u32 v225, 23552, v7
v_add_u32 v226, 47104, v224
v_add_u32 v227, 70656, v7
s_mul_i32 s49, s51, 24576
s_lshr_b32 s52, s69, 1
s_lshl_b32 s52, s52, 10
s_add_u32 s49, s49, s52
s_add_u32 s49, s49, 1024
s_lshl_b32 s64, s65, 3
s_lshl_b32 s66, s69, 12
s_waitcnt lgkmcnt(0)
s_and_b32 s3, s45, 2088960
s_buffer_load_dwordx2 s[46:47], s[12:15], 0x0
buffer_load_dwordx4 v[64:67], v2, s[4:7], s3 offen
buffer_load_dwordx4 v[68:71], v2, s[4:7], s3 offen offset:512
buffer_load_dwordx4 v[72:75], v99, s[8:11], 0 offen
buffer_load_dwordx4 v[76:79], v99, s[8:11], 0 offen offset:512
s_and_b32 s55, s44, 31
s_bfe_u32 s56, s44, 0x30005
s_lshl_b32 s52, s56, 7
s_add_u32 s49, s49, s52
s_mul_i32 s53, s55, 212992
s_add_u32 s64, s64, s53
s_mov_b32 s0, s49
s_add_u32 s1, s49, 1572864
s_bfe_u32 s51, s45, 0x30000
s_mul_i32 s51, s51, 196608
s_add_u32 s49, s49, s51
s_mov_b32 s59, s64
s_add_u32 s68, s64, 1024
s_bfe_u32 s51, s45, 0x30000
s_mul_i32 s51, s51, 128
s_add_u32 s64, s64, s51
s_add_u32 m0, s66, 57344
s_nop 0
buffer_load_dwordx4 v224, s[20:23], s49 offen offset:0 lds
buffer_load_dwordx4 v225, s[20:23], s49 offen offset:1024 lds
buffer_load_dwordx4 v226, s[20:23], s49 offen offset:2048 lds
buffer_load_dwordx4 v227, s[20:23], s49 offen offset:3072 lds
s_add_u32 m0, s65, 0
s_nop 0
buffer_load_dwordx4 v217, s[4:7], s64 offen offset:0 lds
buffer_load_dwordx4 v218, s[4:7], s64 offen offset:1024 lds
buffer_load_dwordx4 v219, s[4:7], s64 offen offset:2048 lds
buffer_load_dwordx4 v220, s[4:7], s64 offen offset:3072 lds
s_add_u32 m0, s65, 4096
s_nop 0
buffer_load_dwordx4 v221, s[4:7], s64 offen offset:0 lds
buffer_load_dwordx4 v222, s[4:7], s64 offen offset:1024 lds
buffer_load_dwordx4 v223, s[4:7], s64 offen offset:2048 lds
s_lshr_b32 s63, s3, 13
s_bfe_u32 s57, s44, 0x50009
s_bfe_u32 s58, s44, 0x3000e
s_add_u32 s50, s45, s69
s_and_b32 s50, s50, 7
s_xor_b32 s60, s50, 4
s_mul_i32 s50, s50, 196608
s_add_u32 s50, s50, s52
s_mul_i32 s60, s60, 196608
s_add_u32 s60, s60, s52
v_and_b32 v1, 15, v240
v_lshrrev_b32 v3, 4, v240
v_lshrrev_b32 v4, 1, v1
v_xor_b32 v4, v3, v4
v_lshlrev_b32 v4, 4, v4
v_lshl_add_u32 v200, v1, 7, v4
s_lshl_b32 s51, s69, 11
v_add_u32 v196, s51, v200
v_xor_b32 v197, 2048, v196
v_xor_b32 v198, 4096, v196
v_xor_b32 v199, 6144, v196
v_xor_b32 v201, 64, v196
v_xor_b32 v202, 64, v197
v_xor_b32 v203, 64, v198
v_xor_b32 v204, 64, v199
v_xor_b32 v205, 64, v200
v_add_u32 v206, 106496, v196
v_add_u32 v207, 106496, v197
v_add_u32 v208, 106496, v198
v_add_u32 v209, 106496, v199
v_add_u32 v210, 106496, v200
v_add_u32 v211, 106496, v201
v_add_u32 v212, 106496, v202
v_add_u32 v213, 106496, v203
v_add_u32 v214, 106496, v204
v_add_u32 v215, 106496, v205
v_bfe_u32 v4, v240, 2, 2
v_lshl_add_u32 v4, v3, 3, v4
v_bfe_u32 v5, v240, 3, 2
v_xor_b32 v5, s69, v5
v_lshlrev_b32 v5, 5, v5
v_lshl_add_u32 v4, v4, 7, v5
v_and_b32 v5, 3, v240
v_lshl_add_u32 v4, v5, 3, v4
v_add_u32 v216, 57344, v4
v_add_u32 v228, 98304, v4
v_xor_b32 v229, 32, v228
v_xor_b32 v230, 64, v228
v_xor_b32 v231, 96, v228
v_mul_u32_u24 v114, 1088, v98
v_add_u32 v114, v114, v99
v_add_u32 v114, 147456, v114
v_bfe_u32 v1, v0, 2, 2
v_mul_u32_u24 v115, 1088, v1
v_lshrrev_b32 v1, 4, v0
v_lshl_add_u32 v115, v1, 5, v115
v_and_b32 v1, 3, v0
v_lshl_add_u32 v115, v1, 3, v115
v_add_u32 v115, 147456, v115
v_and_b32 v1, 1, v98
v_lshl_add_u32 v116, v1, 10, v99
v_lshlrev_b32 v117, 2, v98
s_lshr_b32 s51, s63, 1
s_lshl_b32 s51, s51, 11
s_and_b32 s52, s63, 1
s_lshl_b32 s52, s52, 9
s_add_u32 s51, s51, s52
s_lshl_b32 s52, s69, 18
s_add_u32 s51, s51, s52
s_add_u32 s51, s51, 1048576
s_add_u32 s52, s51, 1048576
s_lshl_b32 s53, s63, 7
s_add_u32 s67, s53, 131072
s_add_u32 s53, s53, 4096
v_mov_b32 v104, 0
s_waitcnt vmcnt(14)
v_lshlrev_b32 v82, 16, v64
v_and_b32 v83, 0xffff0000, v64
v_lshlrev_b32 v84, 16, v65
v_and_b32 v85, 0xffff0000, v65
v_lshlrev_b32 v86, 16, v66
v_and_b32 v87, 0xffff0000, v66
v_lshlrev_b32 v88, 16, v67
v_and_b32 v89, 0xffff0000, v67
s_waitcnt vmcnt(13)
v_lshlrev_b32 v90, 16, v68
v_and_b32 v91, 0xffff0000, v68
v_lshlrev_b32 v92, 16, v69
v_and_b32 v93, 0xffff0000, v69
v_lshlrev_b32 v94, 16, v70
v_and_b32 v95, 0xffff0000, v70
v_lshlrev_b32 v96, 16, v71
v_and_b32 v97, 0xffff0000, v71
v_pk_add_f32 v[100:101], v[82:83], v[84:85]
v_pk_add_f32 v[102:103], v[86:87], v[88:89]
v_pk_add_f32 v[100:101], v[100:101], v[90:91]
v_pk_add_f32 v[102:103], v[102:103], v[92:93]
v_pk_add_f32 v[100:101], v[100:101], v[94:95]
v_pk_add_f32 v[102:103], v[102:103], v[96:97]
v_pk_add_f32 v[100:101], v[100:101], v[102:103]
v_add_f32 v100, v100, v101
s_waitcnt vmcnt(12)
v_dot2c_f32_bf16 v104, v64, v72
v_dot2c_f32_bf16 v104, v65, v73
v_add_f32_dpp v100, v100, v100 row_ror:8 row_mask:0xf bank_mask:0xf
v_dot2c_f32_bf16 v104, v66, v74
v_dot2c_f32_bf16 v104, v67, v75
v_add_f32_dpp v100, v100, v100 row_ror:4 row_mask:0xf bank_mask:0xf
s_waitcnt vmcnt(11)
v_dot2c_f32_bf16 v104, v68, v76
v_dot2c_f32_bf16 v104, v69, v77
v_add_f32_dpp v100, v100, v100 row_ror:2 row_mask:0xf bank_mask:0xf
v_dot2c_f32_bf16 v104, v70, v78
v_dot2c_f32_bf16 v104, v71, v79
v_add_f32_dpp v100, v100, v100 row_ror:1 row_mask:0xf bank_mask:0xf
v_mov_b32 v101, v100
s_nop 1
v_permlane16_swap_b32 v100, v101
v_add_f32 v100, v100, v101
v_mul_f32 v100, 0xbb000000, v100
v_pk_add_f32 v[82:83], v[82:83], v[100:101] op_sel_hi:[1,0]
v_pk_add_f32 v[84:85], v[84:85], v[100:101] op_sel_hi:[1,0]
v_pk_add_f32 v[86:87], v[86:87], v[100:101] op_sel_hi:[1,0]
v_pk_add_f32 v[88:89], v[88:89], v[100:101] op_sel_hi:[1,0]
v_pk_add_f32 v[90:91], v[90:91], v[100:101] op_sel_hi:[1,0]
v_pk_add_f32 v[92:93], v[92:93], v[100:101] op_sel_hi:[1,0]
v_pk_add_f32 v[94:95], v[94:95], v[100:101] op_sel_hi:[1,0]
v_pk_add_f32 v[96:97], v[96:97], v[100:101] op_sel_hi:[1,0]
v_pk_mul_f32 v[102:103], v[82:83], v[82:83]
v_pk_mul_f32 v[100:101], v[84:85], v[84:85]
v_pk_fma_f32 v[102:103], v[86:87], v[86:87], v[102:103]
v_pk_fma_f32 v[100:101], v[88:89], v[88:89], v[100:101]
v_pk_fma_f32 v[102:103], v[90:91], v[90:91], v[102:103]
v_pk_fma_f32 v[100:101], v[92:93], v[92:93], v[100:101]
v_pk_fma_f32 v[102:103], v[94:95], v[94:95], v[102:103]
v_pk_fma_f32 v[100:101], v[96:97], v[96:97], v[100:101]
v_pk_add_f32 v[102:103], v[102:103], v[100:101]
v_add_f32 v102, v102, v103
s_nop 1
v_add_f32_dpp v102, v102, v102 row_ror:8 row_mask:0xf bank_mask:0xf
v_add_f32_dpp v104, v104, v104 row_ror:8 row_mask:0xf bank_mask:0xf
s_nop 0
v_add_f32_dpp v102, v102, v102 row_ror:4 row_mask:0xf bank_mask:0xf
v_add_f32_dpp v104, v104, v104 row_ror:4 row_mask:0xf bank_mask:0xf
s_nop 0
v_add_f32_dpp v102, v102, v102 row_ror:2 row_mask:0xf bank_mask:0xf
v_add_f32_dpp v104, v104, v104 row_ror:2 row_mask:0xf bank_mask:0xf
s_nop 0
v_add_f32_dpp v102, v102, v102 row_ror:1 row_mask:0xf bank_mask:0xf
v_add_f32_dpp v104, v104, v104 row_ror:1 row_mask:0xf bank_mask:0xf
v_mov_b32 v100, v102
v_mov_b32 v101, v104
s_nop 0
v_permlane16_swap_b32 v102, v100
v_permlane16_swap_b32 v104, v101
v_add_f32 v102, v102, v100
v_add_f32 v104, v104, v101
v_mov_b32 v100, 0x3727c5ac
v_fmac_f32 v100, 0x3b000000, v102
v_rsq_f32 v100, v100
v_mul_f32 v104, 0xbfb8aa3b, v104
s_add_u32 m0, s66, 73728
s_add_u32 s49, s49, 196608
s_cmp_eq_u32 s49, s1
s_cselect_b32 s49, s0, s49
buffer_load_dwordx4 v224, s[20:23], s49 offen offset:0 lds
v_pk_mul_f32 v[82:83], v[82:83], v[100:101] op_sel_hi:[1,0]
v_pk_mul_f32 v[84:85], v[84:85], v[100:101] op_sel_hi:[1,0]
v_pk_mul_f32 v[86:87], v[86:87], v[100:101] op_sel_hi:[1,0]
v_pk_mul_f32 v[88:89], v[88:89], v[100:101] op_sel_hi:[1,0]
v_pk_mul_f32 v[90:91], v[90:91], v[100:101] op_sel_hi:[1,0]
v_pk_mul_f32 v[92:93], v[92:93], v[100:101] op_sel_hi:[1,0]
v_pk_mul_f32 v[94:95], v[94:95], v[100:101] op_sel_hi:[1,0]
v_pk_mul_f32 v[96:97], v[96:97], v[100:101] op_sel_hi:[1,0]
v_cvt_pk_bf16_f32 v64, v82, v83
v_cvt_pk_bf16_f32 v65, v84, v85
v_cvt_pk_bf16_f32 v66, v86, v87
v_cvt_pk_bf16_f32 v67, v88, v89
ds_write_b128 v114, v[64:67]
buffer_load_dwordx4 v225, s[20:23], s49 offen offset:1024 lds
v_cvt_pk_bf16_f32 v68, v90, v91
v_cvt_pk_bf16_f32 v69, v92, v93
v_cvt_pk_bf16_f32 v70, v94, v95
v_cvt_pk_bf16_f32 v71, v96, v97
ds_write_b128 v114, v[68:71] offset:512
v_exp_f32 v104, v104
s_nop 0
v_add_f32 v104, 1.0, v104
v_rcp_f32 v104, v104
buffer_load_dwordx4 v226, s[20:23], s49 offen offset:2048 lds
s_waitcnt lgkmcnt(0)
s_barrier
ds_read_b64_tr_b16 v[106:107], v115 offset:0
ds_read_b64_tr_b16 v[108:109], v115 offset:4352
ds_read_b64_tr_b16 v[110:111], v115 offset:512
ds_read_b64_tr_b16 v[112:113], v115 offset:4864
buffer_load_dwordx4 v227, s[20:23], s49 offen offset:3072 lds
s_mov_b32 exec_lo, 1
s_mov_b32 exec_hi, 1
buffer_store_dword v104, v117, s[12:15], s67 offen sc1
s_mov_b64 exec, -1
s_add_u32 s46, s46, 0x9e3779b9
s_add_u32 s47, s47, 0x7f4a7c15
s_waitcnt lgkmcnt(2)
buffer_store_dwordx4 v[106:109], v116, s[12:15], s51 offen sc1
s_waitcnt lgkmcnt(0)
buffer_store_dwordx4 v[110:113], v116, s[12:15], s52 offen sc1
s_mov_b32 s3, s53
s_add_u32 m0, s65, 28672
s_add_u32 s64, s64, 128
s_cmp_eq_u32 s64, s68
s_cselect_b32 s64, s59, s64
buffer_load_dwordx4 v217, s[4:7], s64 offen offset:0 lds
buffer_load_dwordx4 v218, s[4:7], s64 offen offset:1024 lds
buffer_load_dwordx4 v219, s[4:7], s64 offen offset:2048 lds
s_lshl_b32 s51, s56, 2
s_add_u32 s51, s51, s69
s_mul_i32 s63, s51, 10
s_mul_i32 s52, s55, 26
v_min_u32 v1, 25, v240
v_add_u32 v1, s52, v1
v_min_u32 v1, 255, v1
v_add_lshl_u32 v249, v1, 32, 7
s_mul_i32 s51, s55, 208
v_and_b32 v1, 15, v240
v_add_u32 v1, s51, v1
s_lshl_b32 s51, s69, 4
v_bfe_u32 v2, v240, 4, 1
v_lshrrev_b32 v3, 5, v240
v_lshlrev_b32 v3, 9, v3
v_xor_b32 v4, s69, v2
v_lshl_add_u32 v4, v4, 11, v3
s_lshl_b32 s52, s69, 11
v_lshl_add_u32 v5, v2, 3, v3
v_add_u32 v5, s52, v5
v_add_u32 v6, s51, v1
v_lshrrev_b32 v7, 3, v6
v_and_b32 v8, 7, v6
v_lshlrev_b32 v8, 2, v8
v_lshl_add_u32 v245, v7, 7, v8
v_lshrrev_b32 v7, 6, v6
v_lshl_add_u32 v7, v7, 16, v4
v_bfe_u32 v8, v6, 5, 1
v_lshl_add_u32 v7, v8, 10, v7
v_and_b32 v8, 31, v6
v_lshl_add_u32 v232, v8, 4, v7
v_xor_b32 v233, 0x1000, v232
v_add_u32 v6, s51, v1
v_add_u32 v6, 64, v6
v_lshrrev_b32 v7, 3, v6
v_and_b32 v8, 7, v6
v_lshlrev_b32 v8, 2, v8
v_lshl_add_u32 v246, v7, 7, v8
v_lshrrev_b32 v7, 6, v6
v_lshl_add_u32 v7, v7, 16, v4
v_bfe_u32 v8, v6, 5, 1
v_lshl_add_u32 v7, v8, 10, v7
v_and_b32 v8, 31, v6
v_lshl_add_u32 v234, v8, 4, v7
v_xor_b32 v235, 0x1000, v234
v_add_u32 v6, s51, v1
v_add_u32 v6, 128, v6
v_lshrrev_b32 v7, 3, v6
v_and_b32 v8, 7, v6
v_lshlrev_b32 v8, 2, v8
v_lshl_add_u32 v252, v7, 7, v8
v_lshrrev_b32 v7, 6, v6
v_lshl_add_u32 v7, v7, 16, v4
v_bfe_u32 v8, v6, 5, 1
v_lshl_add_u32 v7, v8, 10, v7
v_and_b32 v8, 31, v6
v_lshl_add_u32 v236, v8, 4, v7
v_xor_b32 v237, 0x1000, v236
v_add_u32 v6, 192, v1
v_lshrrev_b32 v7, 3, v6
v_and_b32 v8, 7, v6
v_lshlrev_b32 v8, 2, v8
v_lshl_add_u32 v253, v7, 7, v8
v_lshrrev_b32 v7, 6, v6
v_lshl_add_u32 v7, v7, 16, v5
v_bfe_u32 v8, v6, 5, 1
v_lshl_add_u32 v7, v8, 10, v7
v_and_b32 v8, 31, v6
v_lshl_add_u32 v238, v8, 4, v7
s_waitcnt vmcnt(10)
s_barrier
ds_read_b64_tr_b16 v[128:129], v216 offset:0
ds_read_b64_tr_b16 v[130:131], v216 offset:512
ds_read_b64_tr_b16 v[132:133], v216 offset:8192
ds_read_b64_tr_b16 v[134:135], v216 offset:8704
ds_read_b128 a[0:3], v196 offset:0
ds_read_b128 v[104:107], v197 offset:0
ds_read_b128 v[108:111], v198 offset:0
ds_read_b128 v[112:115], v199 offset:0
ds_read_b128 a[4:7], v196 offset:8192
s_waitcnt lgkmcnt(4)
v_mfma_f32_16x16x32_bf16 v[0:3], a[0:3], v[128:131], 1.0
ds_read_b128 v[116:119], v197 offset:8192
v_mfma_f32_16x16x32_bf16 v[52:55], a[0:3], v[132:135], 0
buffer_load_dwordx4 v220, s[4:7], s64 offen offset:3072 lds
s_waitcnt lgkmcnt(4)
v_mfma_f32_16x16x32_bf16 v[4:7], v[104:107], v[128:131], 1.0
ds_read_b128 v[120:123], v198 offset:8192
v_mfma_f32_16x16x32_bf16 v[56:59], v[104:107], v[132:135], 0
s_waitcnt lgkmcnt(4)
v_mfma_f32_16x16x32_bf16 v[8:11], v[108:111], v[128:131], 1.0
ds_read_b128 v[124:127], v199 offset:8192
v_mfma_f32_16x16x32_bf16 v[60:63], v[108:111], v[132:135], 0
s_add_u32 m0, s65, 32768
s_nop 0
buffer_load_dwordx4 v221, s[4:7], s64 offen offset:0 lds
s_mov_b32 s52, 0x3f803f80
s_waitcnt lgkmcnt(4)
v_mfma_f32_16x16x32_bf16 v[12:15], v[112:115], v[128:131], 1.0
ds_read_b128 a[8:11], v196 offset:16384
v_mfma_f32_16x16x32_bf16 v[64:67], v[112:115], v[132:135], 0
v_mov_b32 v144, s52
s_waitcnt lgkmcnt(4)
v_mfma_f32_16x16x32_bf16 v[16:19], a[4:7], v[128:131], 1.0
ds_read_b128 v[104:107], v197 offset:16384
v_mfma_f32_16x16x32_bf16 v[68:71], a[4:7], v[132:135], 0
buffer_load_dwordx4 v222, s[4:7], s64 offen offset:1024 lds
v_mov_b32 v145, s52
s_waitcnt lgkmcnt(4)
v_mfma_f32_16x16x32_bf16 v[20:23], v[116:119], v[128:131], 1.0
ds_read_b128 v[108:111], v198 offset:16384
v_mfma_f32_16x16x32_bf16 v[72:75], v[116:119], v[132:135], 0
v_mov_b32 v146, s52
s_waitcnt lgkmcnt(4)
v_mfma_f32_16x16x32_bf16 v[24:27], v[120:123], v[128:131], 1.0
ds_read_b128 v[112:115], v199 offset:16384
v_mfma_f32_16x16x32_bf16 v[76:79], v[120:123], v[132:135], 0
buffer_load_dwordx4 v223, s[4:7], s64 offen offset:2048 lds
v_mov_b32 v147, s52
s_waitcnt lgkmcnt(4)
v_mfma_f32_16x16x32_bf16 v[28:31], v[124:127], v[128:131], 1.0
ds_read_b128 a[12:15], v200 offset:24576
v_mfma_f32_16x16x32_bf16 v[80:83], v[124:127], v[132:135], 0
s_cmp_eq_u32 s55, 9
s_cselect_b32 s51, s69, 7
s_cselect_b32 s67, 0, s52
s_waitcnt lgkmcnt(4)
v_mfma_f32_16x16x32_bf16 v[32:35], a[8:11], v[128:131], 1.0
ds_read_b128 a[16:19], v201 offset:0
v_mfma_f32_16x16x32_bf16 v[84:87], a[8:11], v[132:135], 0
ds_read_b64_tr_b16 v[136:137], v216 offset:4096
ds_read_b64_tr_b16 v[138:139], v216 offset:4608
v_mov_b32 v156, s67
s_waitcnt lgkmcnt(6)
v_mfma_f32_16x16x32_bf16 v[36:39], v[104:107], v[128:131], 1.0
ds_read_b128 v[116:119], v202 offset:0
v_mfma_f32_16x16x32_bf16 v[88:91], v[104:107], v[132:135], 0
ds_read_b64_tr_b16 v[140:141], v216 offset:12288
ds_read_b64_tr_b16 v[142:143], v216 offset:12800
v_mov_b32 v157, s67
s_waitcnt vmcnt(7)
s_barrier
s_cmp_lg_u32 s69, 0
s_cbranch_scc1 FL_2
v_mov_b32 v250, s46
v_mov_b32 v251, s47
s_mov_b64 exec, 1
buffer_store_dwordx2 v[250:251], off, s[12:15], s3 sc1
s_mov_b64 exec, -1
FL_2:
s_waitcnt lgkmcnt(8)
v_mfma_f32_16x16x32_bf16 v[40:43], v[108:111], v[128:131], 1.0
ds_read_b128 v[120:123], v203 offset:0
v_mfma_f32_16x16x32_bf16 v[92:95], v[108:111], v[132:135], 0
s_add_u32 m0, s66, 90112
s_add_u32 s49, s49, 196608
s_cmp_eq_u32 s49, s1
s_cselect_b32 s49, s0, s49
buffer_load_dwordx4 v224, s[20:23], s49 offen offset:0 lds
v_mov_b32 v158, 0
s_waitcnt lgkmcnt(8)
v_mfma_f32_16x16x32_bf16 v[44:47], v[112:115], v[128:131], 1.0
ds_read_b128 v[124:127], v204 offset:0
v_mfma_f32_16x16x32_bf16 v[96:99], v[112:115], v[132:135], 0
v_mov_b32 v159, 0
s_waitcnt lgkmcnt(8)
v_mfma_f32_16x16x32_bf16 v[48:51], a[12:15], v[128:131], 1.0
ds_read_b128 a[20:23], v201 offset:8192
v_mfma_f32_16x16x32_bf16 v[100:103], a[12:15], v[132:135], 0
buffer_load_dwordx4 v225, s[20:23], s49 offen offset:1024 lds
s_cmp_eq_u32 s51, 3
s_cselect_b32 s67, 0, s52
s_waitcnt lgkmcnt(6)
v_mfma_f32_16x16x32_bf16 v[0:3], a[16:19], v[136:139], v[0:3]
ds_read_b128 v[104:107], v202 offset:8192
s_waitcnt lgkmcnt(4)
v_mfma_f32_16x16x32_bf16 v[52:55], a[16:19], v[140:143], v[52:55]
v_mov_b32 v148, s67
v_mfma_f32_16x16x32_bf16 v[4:7], v[116:119], v[136:139], v[4:7]
ds_read_b128 v[108:111], v203 offset:8192
v_mfma_f32_16x16x32_bf16 v[56:59], v[116:119], v[140:143], v[56:59]
buffer_load_dwordx4 v226, s[20:23], s49 offen offset:2048 lds
v_mov_b32 v149, s67
s_waitcnt lgkmcnt(4)
v_mfma_f32_16x16x32_bf16 v[8:11], v[120:123], v[136:139], v[8:11]
ds_read_b128 v[112:115], v204 offset:8192
v_mfma_f32_16x16x32_bf16 v[60:63], v[120:123], v[140:143], v[60:63]
s_cmp_eq_u32 s51, 2
s_cselect_b32 s67, 0, s52
s_waitcnt lgkmcnt(4)
v_mfma_f32_16x16x32_bf16 v[12:15], v[124:127], v[136:139], v[12:15]
ds_read_b128 a[24:27], v201 offset:16384
v_mfma_f32_16x16x32_bf16 v[64:67], v[124:127], v[140:143], v[64:67]
buffer_load_dwordx4 v227, s[20:23], s49 offen offset:3072 lds
v_mov_b32 v150, s67
s_waitcnt lgkmcnt(4)
v_mfma_f32_16x16x32_bf16 v[16:19], a[20:23], v[136:139], v[16:19]
ds_read_b128 v[116:119], v202 offset:16384
v_mfma_f32_16x16x32_bf16 v[68:71], a[20:23], v[140:143], v[68:71]
v_mov_b32 v151, s67
s_waitcnt lgkmcnt(4)
v_mfma_f32_16x16x32_bf16 v[20:23], v[104:107], v[136:139], v[20:23]
ds_read_b128 v[120:123], v203 offset:16384
v_mfma_f32_16x16x32_bf16 v[72:75], v[104:107], v[140:143], v[72:75]
s_add_u32 m0, s65, 106496
s_add_u32 s64, s64, 128
s_cmp_eq_u32 s64, s68
s_cselect_b32 s64, s59, s64
buffer_load_dwordx4 v217, s[4:7], s64 offen offset:0 lds
s_cmp_eq_u32 s51, 1
s_cselect_b32 s67, 0, s52
s_waitcnt lgkmcnt(4)
v_mfma_f32_16x16x32_bf16 v[24:27], v[108:111], v[136:139], v[24:27]
ds_read_b128 v[124:127], v204 offset:16384
v_mfma_f32_16x16x32_bf16 v[76:79], v[108:111], v[140:143], v[76:79]
v_mov_b32 v152, s67
s_waitcnt lgkmcnt(4)
v_mfma_f32_16x16x32_bf16 v[28:31], v[112:115], v[136:139], v[28:31]
ds_read_b128 a[28:31], v205 offset:24576
v_mfma_f32_16x16x32_bf16 v[80:83], v[112:115], v[140:143], v[80:83]
buffer_load_dwordx4 v218, s[4:7], s64 offen offset:1024 lds
v_mov_b32 v153, s67
s_waitcnt lgkmcnt(4)
v_mfma_f32_16x16x32_bf16 v[32:35], a[24:27], v[136:139], v[32:35]
s_waitcnt vmcnt(6)
s_barrier
ds_read_b128 a[32:35], v196 offset:28672
v_mfma_f32_16x16x32_bf16 v[84:87], a[24:27], v[140:143], v[84:87]
ds_read_b64_tr_b16 v[128:129], v216 offset:16384
ds_read_b64_tr_b16 v[130:131], v216 offset:16896
s_cmp_eq_u32 s51, 0
s_cselect_b32 s67, 0, s52
s_waitcnt lgkmcnt(6)
v_mfma_f32_16x16x32_bf16 v[36:39], v[116:119], v[136:139], v[36:39]
ds_read_b128 v[104:107], v197 offset:28672
v_mfma_f32_16x16x32_bf16 v[88:91], v[116:119], v[140:143], v[88:91]
ds_read_b64_tr_b16 v[132:133], v216 offset:24576
ds_read_b64_tr_b16 v[134:135], v216 offset:25088
v_mov_b32 v154, s67
s_waitcnt lgkmcnt(8)
v_mfma_f32_16x16x32_bf16 v[40:43], v[120:123], v[136:139], v[40:43]
ds_read_b128 v[108:111], v198 offset:28672
v_mfma_f32_16x16x32_bf16 v[92:95], v[120:123], v[140:143], v[92:95]
v_mov_b32 v155, s67
s_waitcnt lgkmcnt(8)
v_mfma_f32_16x16x32_bf16 v[44:47], v[124:127], v[136:139], v[44:47]
ds_read_b128 v[112:115], v199 offset:28672
v_mfma_f32_16x16x32_bf16 v[96:99], v[124:127], v[140:143], v[96:99]
buffer_load_dwordx4 v219, s[4:7], s64 offen offset:2048 lds
v_and_b32 v176, 48, v240
s_waitcnt lgkmcnt(8)
v_mfma_f32_16x16x32_bf16 v[48:51], a[28:31], v[136:139], v[48:51]
ds_read_b128 a[36:39], v196 offset:36864
v_mfma_f32_16x16x32_bf16 v[100:103], a[28:31], v[140:143], v[100:103]
v_mov_b32 v173, s46
s_barrier
s_waitcnt lgkmcnt(6)
v_mfma_f32_16x16x32_bf16 v[0:3], a[32:35], v[128:131], v[0:3]
ds_read_b128 v[116:119], v197 offset:36864
s_waitcnt lgkmcnt(4)
v_mfma_f32_16x16x32_bf16 v[52:55], a[32:35], v[132:135], v[52:55]
buffer_load_dwordx4 v220, s[4:7], s64 offen offset:3072 lds
v_mov_b32 v175, s47
v_mfma_f32_16x16x32_bf16 v[4:7], v[104:107], v[128:131], v[4:7]
ds_read_b128 v[120:123], v198 offset:36864
v_mfma_f32_16x16x32_bf16 v[56:59], v[104:107], v[132:135], v[56:59]
s_add_u32 m0, s65, 110592
s_nop 0
buffer_load_dwordx4 v221, s[4:7], s64 offen offset:0 lds
v_mov_b32 v169, s46
s_waitcnt lgkmcnt(4)
v_mfma_f32_16x16x32_bf16 v[8:11], v[108:111], v[128:131], v[8:11]
ds_read_b128 v[124:127], v199 offset:36864
v_mfma_f32_16x16x32_bf16 v[60:63], v[108:111], v[132:135], v[60:63]
buffer_load_dwordx4 v222, s[4:7], s64 offen offset:1024 lds
v_mov_b32 v171, s47
s_waitcnt lgkmcnt(4)
v_mfma_f32_16x16x32_bf16 v[12:15], v[112:115], v[128:131], v[12:15]
ds_read_b128 a[40:43], v196 offset:45056
v_mfma_f32_16x16x32_bf16 v[64:67], v[112:115], v[132:135], v[64:67]
buffer_load_dwordx4 v223, s[4:7], s64 offen offset:2048 lds
v_and_b32 v239, 15, v240
s_waitcnt lgkmcnt(4)
v_mfma_f32_16x16x32_bf16 v[16:19], a[36:39], v[128:131], v[16:19]
ds_read_b128 v[104:107], v197 offset:45056
v_mfma_f32_16x16x32_bf16 v[68:71], a[36:39], v[132:135], v[68:71]
s_add_u32 m0, s66, 57344
s_add_u32 s49, s49, 196608
s_cmp_eq_u32 s49, s1
s_cselect_b32 s49, s0, s49
buffer_load_dwordx4 v224, s[20:23], s49 offen offset:0 lds
v_min_u32 v239, 9, v239
s_waitcnt lgkmcnt(4)
v_mfma_f32_16x16x32_bf16 v[20:23], v[116:119], v[128:131], v[20:23]
ds_read_b128 v[108:111], v198 offset:45056
v_mfma_f32_16x16x32_bf16 v[72:75], v[116:119], v[132:135], v[72:75]
v_lshl_add_u32 v239, v239, 11, v176
s_waitcnt lgkmcnt(4)
v_mfma_f32_16x16x32_bf16 v[24:27], v[120:123], v[128:131], v[24:27]
ds_read_b128 v[112:115], v199 offset:45056
v_mfma_f32_16x16x32_bf16 v[76:79], v[120:123], v[132:135], v[76:79]
buffer_load_dwordx4 v225, s[20:23], s49 offen offset:1024 lds
s_mov_b32 s67, 131072
s_waitcnt lgkmcnt(4)
v_mfma_f32_16x16x32_bf16 v[28:31], v[124:127], v[128:131], v[28:31]
ds_read_b128 a[44:47], v200 offset:53248
v_mfma_f32_16x16x32_bf16 v[80:83], v[124:127], v[132:135], v[80:83]
s_add_u32 s53, s63, s55
s_waitcnt lgkmcnt(4)
v_mfma_f32_16x16x32_bf16 v[32:35], a[40:43], v[128:131], v[32:35]
ds_read_b128 a[48:51], v201 offset:28672
v_mfma_f32_16x16x32_bf16 v[84:87], a[40:43], v[132:135], v[84:87]
ds_read_b64_tr_b16 v[136:137], v216 offset:20480
ds_read_b64_tr_b16 v[138:139], v216 offset:20992
s_lshl_b32 s53, s53, 11
s_waitcnt lgkmcnt(6)
v_mfma_f32_16x16x32_bf16 v[36:39], v[104:107], v[128:131], v[36:39]
ds_read_b128 v[116:119], v202 offset:28672
v_mfma_f32_16x16x32_bf16 v[88:91], v[104:107], v[132:135], v[88:91]
ds_read_b64_tr_b16 v[140:141], v216 offset:28672
ds_read_b64_tr_b16 v[142:143], v216 offset:29184
s_add_u32 s53, s53, 262144
s_waitcnt lgkmcnt(8)
v_mfma_f32_16x16x32_bf16 v[40:43], v[108:111], v[128:131], v[40:43]
ds_read_b128 v[120:123], v203 offset:28672
v_mfma_f32_16x16x32_bf16 v[92:95], v[108:111], v[132:135], v[92:95]
buffer_load_dwordx4 v226, s[20:23], s49 offen offset:2048 lds
s_waitcnt lgkmcnt(8)
v_mfma_f32_16x16x32_bf16 v[44:47], v[112:115], v[128:131], v[44:47]
ds_read_b128 v[124:127], v204 offset:28672
v_mfma_f32_16x16x32_bf16 v[96:99], v[112:115], v[132:135], v[96:99]
s_waitcnt lgkmcnt(8)
v_mfma_f32_16x16x32_bf16 v[48:51], a[44:47], v[128:131], v[48:51]
ds_read_b128 a[52:55], v201 offset:36864
v_mfma_f32_16x16x32_bf16 v[100:103], a[44:47], v[132:135], v[100:103]
buffer_load_dwordx4 v227, s[20:23], s49 offen offset:3072 lds
s_waitcnt lgkmcnt(6)
v_mfma_f32_16x16x32_bf16 v[0:3], a[48:51], v[136:139], v[0:3]
ds_read_b128 v[104:107], v202 offset:36864
s_waitcnt lgkmcnt(4)
v_mfma_f32_16x16x32_bf16 v[52:55], a[48:51], v[140:143], v[52:55]
v_mfma_f32_16x16x32_bf16 v[4:7], v[116:119], v[136:139], v[4:7]
ds_read_b128 v[108:111], v203 offset:36864
v_mfma_f32_16x16x32_bf16 v[56:59], v[116:119], v[140:143], v[56:59]
s_add_u32 m0, s65, 0
s_add_u32 s64, s64, 128
s_cmp_eq_u32 s64, s68
s_cselect_b32 s64, s59, s64
buffer_load_dwordx4 v217, s[4:7], s64 offen offset:0 lds
s_waitcnt lgkmcnt(4)
v_mfma_f32_16x16x32_bf16 v[8:11], v[120:123], v[136:139], v[8:11]
ds_read_b128 v[112:115], v204 offset:36864
v_mfma_f32_16x16x32_bf16 v[60:63], v[120:123], v[140:143], v[60:63]
s_waitcnt lgkmcnt(4)
v_mfma_f32_16x16x32_bf16 v[12:15], v[124:127], v[136:139], v[12:15]
ds_read_b128 a[56:59], v201 offset:45056
v_mfma_f32_16x16x32_bf16 v[64:67], v[124:127], v[140:143], v[64:67]
buffer_load_dwordx4 v218, s[4:7], s64 offen offset:1024 lds
s_waitcnt lgkmcnt(4)
v_mfma_f32_16x16x32_bf16 v[16:19], a[52:55], v[136:139], v[16:19]
ds_read_b128 v[116:119], v202 offset:45056
v_mfma_f32_16x16x32_bf16 v[68:71], a[52:55], v[140:143], v[68:71]
s_waitcnt lgkmcnt(4)
v_mfma_f32_16x16x32_bf16 v[20:23], v[104:107], v[136:139], v[20:23]
ds_read_b128 v[120:123], v203 offset:45056
v_mfma_f32_16x16x32_bf16 v[72:75], v[104:107], v[140:143], v[72:75]
buffer_load_dwordx4 v219, s[4:7], s64 offen offset:2048 lds
s_waitcnt lgkmcnt(4)
v_mfma_f32_16x16x32_bf16 v[24:27], v[108:111], v[136:139], v[24:27]
ds_read_b128 v[124:127], v204 offset:45056
v_mfma_f32_16x16x32_bf16 v[76:79], v[108:111], v[140:143], v[76:79]
s_waitcnt lgkmcnt(4)
v_mfma_f32_16x16x32_bf16 v[28:31], v[112:115], v[136:139], v[28:31]
ds_read_b128 a[60:63], v205 offset:53248
v_mfma_f32_16x16x32_bf16 v[80:83], v[112:115], v[140:143], v[80:83]
buffer_load_dwordx4 v220, s[4:7], s64 offen offset:3072 lds
s_waitcnt lgkmcnt(4)
v_mfma_f32_16x16x32_bf16 v[32:35], a[56:59], v[136:139], v[32:35]
s_waitcnt vmcnt(8)
s_barrier
ds_read_b128 a[64:67], v206 offset:0
v_mfma_f32_16x16x32_bf16 v[84:87], a[56:59], v[140:143], v[84:87]
ds_read_b64_tr_b16 v[128:129], v216 offset:32768
ds_read_b64_tr_b16 v[130:131], v216 offset:33280
s_waitcnt lgkmcnt(6)
v_mfma_f32_16x16x32_bf16 v[36:39], v[116:119], v[136:139], v[36:39]
ds_read_b128 v[104:107], v207 offset:0
v_mfma_f32_16x16x32_bf16 v[88:91], v[116:119], v[140:143], v[88:91]
ds_read_b64_tr_b16 v[132:133], v216 offset:40960
ds_read_b64_tr_b16 v[134:135], v216 offset:41472
s_waitcnt lgkmcnt(8)
v_mfma_f32_16x16x32_bf16 v[40:43], v[120:123], v[136:139], v[40:43]
ds_read_b128 v[108:111], v208 offset:0
v_mfma_f32_16x16x32_bf16 v[92:95], v[120:123], v[140:143], v[92:95]
s_waitcnt lgkmcnt(8)
v_mfma_f32_16x16x32_bf16 v[44:47], v[124:127], v[136:139], v[44:47]
ds_read_b128 v[112:115], v209 offset:0
v_mfma_f32_16x16x32_bf16 v[96:99], v[124:127], v[140:143], v[96:99]
s_add_u32 m0, s65, 4096
s_nop 0
buffer_load_dwordx4 v221, s[4:7], s64 offen offset:0 lds
s_waitcnt lgkmcnt(8)
v_mfma_f32_16x16x32_bf16 v[48:51], a[60:63], v[136:139], v[48:51]
ds_read_b128 a[68:71], v206 offset:8192
v_mfma_f32_16x16x32_bf16 v[100:103], a[60:63], v[140:143], v[100:103]
s_barrier
s_waitcnt lgkmcnt(6)
v_mfma_f32_16x16x32_bf16 v[0:3], a[64:67], v[128:131], v[0:3]
ds_read_b128 v[116:119], v207 offset:8192
s_waitcnt lgkmcnt(4)
v_mfma_f32_16x16x32_bf16 v[52:55], a[64:67], v[132:135], v[52:55]
buffer_load_dwordx4 v222, s[4:7], s64 offen offset:1024 lds
v_mfma_f32_16x16x32_bf16 v[4:7], v[104:107], v[128:131], v[4:7]
ds_read_b128 v[120:123], v208 offset:8192
v_mfma_f32_16x16x32_bf16 v[56:59], v[104:107], v[132:135], v[56:59]
buffer_load_dwordx4 v223, s[4:7], s64 offen offset:2048 lds
s_waitcnt lgkmcnt(4)
v_mfma_f32_16x16x32_bf16 v[8:11], v[108:111], v[128:131], v[8:11]
ds_read_b128 v[124:127], v209 offset:8192
v_mfma_f32_16x16x32_bf16 v[60:63], v[108:111], v[132:135], v[60:63]
s_add_u32 m0, s66, 73728
s_add_u32 s49, s49, 196608
s_cmp_eq_u32 s49, s1
s_cselect_b32 s49, s0, s49
buffer_load_dwordx4 v224, s[20:23], s49 offen offset:0 lds
s_waitcnt lgkmcnt(4)
v_mfma_f32_16x16x32_bf16 v[12:15], v[112:115], v[128:131], v[12:15]
ds_read_b128 a[72:75], v206 offset:16384
v_mfma_f32_16x16x32_bf16 v[64:67], v[112:115], v[132:135], v[64:67]
s_waitcnt lgkmcnt(4)
v_mfma_f32_16x16x32_bf16 v[16:19], a[68:71], v[128:131], v[16:19]
ds_read_b128 v[104:107], v207 offset:16384
v_mfma_f32_16x16x32_bf16 v[68:71], a[68:71], v[132:135], v[68:71]
buffer_load_dwordx4 v225, s[20:23], s49 offen offset:1024 lds
s_waitcnt lgkmcnt(4)
v_mfma_f32_16x16x32_bf16 v[20:23], v[116:119], v[128:131], v[20:23]
ds_read_b128 v[108:111], v208 offset:16384
v_mfma_f32_16x16x32_bf16 v[72:75], v[116:119], v[132:135], v[72:75]
s_waitcnt lgkmcnt(4)
v_mfma_f32_16x16x32_bf16 v[24:27], v[120:123], v[128:131], v[24:27]
ds_read_b128 v[112:115], v209 offset:16384
v_mfma_f32_16x16x32_bf16 v[76:79], v[120:123], v[132:135], v[76:79]
buffer_load_dwordx4 v226, s[20:23], s49 offen offset:2048 lds
s_waitcnt lgkmcnt(4)
v_mfma_f32_16x16x32_bf16 v[28:31], v[124:127], v[128:131], v[28:31]
ds_read_b128 a[76:79], v210 offset:24576
v_mfma_f32_16x16x32_bf16 v[80:83], v[124:127], v[132:135], v[80:83]
s_waitcnt lgkmcnt(4)
v_mfma_f32_16x16x32_bf16 v[32:35], a[72:75], v[128:131], v[32:35]
ds_read_b128 a[80:83], v211 offset:0
v_mfma_f32_16x16x32_bf16 v[84:87], a[72:75], v[132:135], v[84:87]
ds_read_b64_tr_b16 v[136:137], v216 offset:36864
ds_read_b64_tr_b16 v[138:139], v216 offset:37376
s_waitcnt lgkmcnt(6)
v_mfma_f32_16x16x32_bf16 v[36:39], v[104:107], v[128:131], v[36:39]
ds_read_b128 v[116:119], v212 offset:0
v_mfma_f32_16x16x32_bf16 v[88:91], v[104:107], v[132:135], v[88:91]
ds_read_b64_tr_b16 v[140:141], v216 offset:45056
ds_read_b64_tr_b16 v[142:143], v216 offset:45568
s_waitcnt lgkmcnt(8)
v_mfma_f32_16x16x32_bf16 v[40:43], v[108:111], v[128:131], v[40:43]
ds_read_b128 v[120:123], v213 offset:0
v_mfma_f32_16x16x32_bf16 v[92:95], v[108:111], v[132:135], v[92:95]
buffer_load_dwordx4 v227, s[20:23], s49 offen offset:3072 lds
s_waitcnt lgkmcnt(8)
v_mfma_f32_16x16x32_bf16 v[44:47], v[112:115], v[128:131], v[44:47]
ds_read_b128 v[124:127], v214 offset:0
v_mfma_f32_16x16x32_bf16 v[96:99], v[112:115], v[132:135], v[96:99]
s_waitcnt lgkmcnt(8)
v_mfma_f32_16x16x32_bf16 v[48:51], a[76:79], v[128:131], v[48:51]
ds_read_b128 a[84:87], v211 offset:8192
v_mfma_f32_16x16x32_bf16 v[100:103], a[76:79], v[132:135], v[100:103]
s_add_u32 m0, s65, 28672
s_add_u32 s64, s64, 128
s_cmp_eq_u32 s64, s68
s_cselect_b32 s64, s59, s64
buffer_load_dwordx4 v217, s[4:7], s64 offen offset:0 lds
s_waitcnt lgkmcnt(6)
v_mfma_f32_16x16x32_bf16 v[0:3], a[80:83], v[136:139], v[0:3]
ds_read_b128 v[104:107], v212 offset:8192
s_waitcnt lgkmcnt(4)
v_mfma_f32_16x16x32_bf16 v[52:55], a[80:83], v[140:143], v[52:55]
v_mfma_f32_16x16x32_bf16 v[4:7], v[116:119], v[136:139], v[4:7]
ds_read_b128 v[108:111], v213 offset:8192
v_mfma_f32_16x16x32_bf16 v[56:59], v[116:119], v[140:143], v[56:59]
buffer_load_dwordx4 v218, s[4:7], s64 offen offset:1024 lds
s_waitcnt lgkmcnt(4)
v_mfma_f32_16x16x32_bf16 v[8:11], v[120:123], v[136:139], v[8:11]
ds_read_b128 v[112:115], v214 offset:8192
v_mfma_f32_16x16x32_bf16 v[60:63], v[120:123], v[140:143], v[60:63]
s_waitcnt lgkmcnt(4)
v_mfma_f32_16x16x32_bf16 v[12:15], v[124:127], v[136:139], v[12:15]
ds_read_b128 a[88:91], v211 offset:16384
v_mfma_f32_16x16x32_bf16 v[64:67], v[124:127], v[140:143], v[64:67]
buffer_load_dwordx4 v219, s[4:7], s64 offen offset:2048 lds
s_waitcnt lgkmcnt(4)
v_mfma_f32_16x16x32_bf16 v[16:19], a[84:87], v[136:139], v[16:19]
ds_read_b128 v[116:119], v212 offset:16384
v_mfma_f32_16x16x32_bf16 v[68:71], a[84:87], v[140:143], v[68:71]
s_waitcnt lgkmcnt(4)
v_mfma_f32_16x16x32_bf16 v[20:23], v[104:107], v[136:139], v[20:23]
ds_read_b128 v[120:123], v213 offset:16384
v_mfma_f32_16x16x32_bf16 v[72:75], v[104:107], v[140:143], v[72:75]
buffer_load_dwordx4 v220, s[4:7], s64 offen offset:3072 lds
s_waitcnt lgkmcnt(4)
v_mfma_f32_16x16x32_bf16 v[24:27], v[108:111], v[136:139], v[24:27]
ds_read_b128 v[124:127], v214 offset:16384
v_mfma_f32_16x16x32_bf16 v[76:79], v[108:111], v[140:143], v[76:79]
s_waitcnt lgkmcnt(4)
v_mfma_f32_16x16x32_bf16 v[28:31], v[112:115], v[136:139], v[28:31]
ds_read_b128 a[92:95], v215 offset:24576
v_mfma_f32_16x16x32_bf16 v[80:83], v[112:115], v[140:143], v[80:83]
s_add_u32 m0, s65, 32768
s_nop 0
buffer_load_dwordx4 v221, s[4:7], s64 offen offset:0 lds
s_waitcnt lgkmcnt(4)
v_mfma_f32_16x16x32_bf16 v[32:35], a[88:91], v[136:139], v[32:35]
s_waitcnt vmcnt(9)
s_barrier
ds_read_b128 a[96:99], v196 offset:0
v_mfma_f32_16x16x32_bf16 v[84:87], a[88:91], v[140:143], v[84:87]
ds_read_b64_tr_b16 v[128:129], v216 offset:0
ds_read_b64_tr_b16 v[130:131], v216 offset:512
s_waitcnt lgkmcnt(6)
v_mfma_f32_16x16x32_bf16 v[36:39], v[116:119], v[136:139], v[36:39]
ds_read_b128 v[104:107], v197 offset:0
v_mfma_f32_16x16x32_bf16 v[88:91], v[116:119], v[140:143], v[88:91]
ds_read_b64_tr_b16 v[132:133], v216 offset:8192
ds_read_b64_tr_b16 v[134:135], v216 offset:8704
s_waitcnt lgkmcnt(8)
v_mfma_f32_16x16x32_bf16 v[40:43], v[120:123], v[136:139], v[40:43]
ds_read_b128 v[108:111], v198 offset:0
v_mfma_f32_16x16x32_bf16 v[92:95], v[120:123], v[140:143], v[92:95]
s_waitcnt lgkmcnt(8)
v_mfma_f32_16x16x32_bf16 v[44:47], v[124:127], v[136:139], v[44:47]
ds_read_b128 v[112:115], v199 offset:0
v_mfma_f32_16x16x32_bf16 v[96:99], v[124:127], v[140:143], v[96:99]
buffer_load_dwordx4 v222, s[4:7], s64 offen offset:1024 lds
s_waitcnt lgkmcnt(8)
v_mfma_f32_16x16x32_bf16 v[48:51], a[92:95], v[136:139], v[48:51]
ds_read_b128 a[100:103], v196 offset:8192
v_mfma_f32_16x16x32_bf16 v[100:103], a[92:95], v[140:143], v[100:103]
s_barrier
s_waitcnt lgkmcnt(6)
v_mfma_f32_16x16x32_bf16 v[0:3], a[96:99], v[128:131], v[0:3]
ds_read_b128 v[116:119], v197 offset:8192
s_waitcnt lgkmcnt(4)
v_mfma_f32_16x16x32_bf16 v[52:55], a[96:99], v[132:135], v[52:55]
buffer_load_dwordx4 v223, s[4:7], s64 offen offset:2048 lds
v_mfma_f32_16x16x32_bf16 v[4:7], v[104:107], v[128:131], v[4:7]
ds_read_b128 v[120:123], v198 offset:8192
v_mfma_f32_16x16x32_bf16 v[56:59], v[104:107], v[132:135], v[56:59]
s_add_u32 m0, s66, 90112
s_add_u32 s49, s49, 196608
s_cmp_eq_u32 s49, s1
s_cselect_b32 s49, s0, s49
buffer_load_dwordx4 v224, s[20:23], s49 offen offset:0 lds
s_waitcnt lgkmcnt(4)
v_mfma_f32_16x16x32_bf16 v[8:11], v[108:111], v[128:131], v[8:11]
ds_read_b128 v[124:127], v199 offset:8192
v_mfma_f32_16x16x32_bf16 v[60:63], v[108:111], v[132:135], v[60:63]
buffer_load_dwordx4 v225, s[20:23], s49 offen offset:1024 lds
s_waitcnt lgkmcnt(4)
v_mfma_f32_16x16x32_bf16 v[12:15], v[112:115], v[128:131], v[12:15]
ds_read_b128 a[104:107], v196 offset:16384
v_mfma_f32_16x16x32_bf16 v[64:67], v[112:115], v[132:135], v[64:67]
s_waitcnt lgkmcnt(4)
v_mfma_f32_16x16x32_bf16 v[16:19], a[100:103], v[128:131], v[16:19]
ds_read_b128 v[104:107], v197 offset:16384
v_mfma_f32_16x16x32_bf16 v[68:71], a[100:103], v[132:135], v[68:71]
buffer_load_dwordx4 v226, s[20:23], s49 offen offset:2048 lds
s_waitcnt lgkmcnt(4)
v_mfma_f32_16x16x32_bf16 v[20:23], v[116:119], v[128:131], v[20:23]
ds_read_b128 v[108:111], v198 offset:16384
v_mfma_f32_16x16x32_bf16 v[72:75], v[116:119], v[132:135], v[72:75]
s_waitcnt lgkmcnt(4)
v_mfma_f32_16x16x32_bf16 v[24:27], v[120:123], v[128:131], v[24:27]
ds_read_b128 v[112:115], v199 offset:16384
v_mfma_f32_16x16x32_bf16 v[76:79], v[120:123], v[132:135], v[76:79]
buffer_load_dwordx4 v227, s[20:23], s49 offen offset:3072 lds
s_waitcnt lgkmcnt(4)
v_mfma_f32_16x16x32_bf16 v[28:31], v[124:127], v[128:131], v[28:31]
ds_read_b128 a[108:111], v200 offset:24576
v_mfma_f32_16x16x32_bf16 v[80:83], v[124:127], v[132:135], v[80:83]
s_waitcnt lgkmcnt(4)
v_mfma_f32_16x16x32_bf16 v[32:35], a[104:107], v[128:131], v[32:35]
ds_read_b128 a[112:115], v201 offset:0
v_mfma_f32_16x16x32_bf16 v[84:87], a[104:107], v[132:135], v[84:87]
ds_read_b64_tr_b16 v[136:137], v216 offset:4096
ds_read_b64_tr_b16 v[138:139], v216 offset:4608
s_waitcnt lgkmcnt(6)
v_mfma_f32_16x16x32_bf16 v[36:39], v[104:107], v[128:131], v[36:39]
ds_read_b128 v[116:119], v202 offset:0
v_mfma_f32_16x16x32_bf16 v[88:91], v[104:107], v[132:135], v[88:91]
ds_read_b64_tr_b16 v[140:141], v216 offset:12288
ds_read_b64_tr_b16 v[142:143], v216 offset:12800
s_waitcnt lgkmcnt(8)
v_mfma_f32_16x16x32_bf16 v[40:43], v[108:111], v[128:131], v[40:43]
ds_read_b128 v[120:123], v203 offset:0
v_mfma_f32_16x16x32_bf16 v[92:95], v[108:111], v[132:135], v[92:95]
s_add_u32 m0, s65, 106496
s_add_u32 s64, s64, 128
s_cmp_eq_u32 s64, s68
s_cselect_b32 s64, s59, s64
buffer_load_dwordx4 v217, s[4:7], s64 offen offset:0 lds
s_waitcnt lgkmcnt(8)
v_mfma_f32_16x16x32_bf16 v[44:47], v[112:115], v[128:131], v[44:47]
ds_read_b128 v[124:127], v204 offset:0
v_mfma_f32_16x16x32_bf16 v[96:99], v[112:115], v[132:135], v[96:99]
s_waitcnt lgkmcnt(8)
v_mfma_f32_16x16x32_bf16 v[48:51], a[108:111], v[128:131], v[48:51]
ds_read_b128 a[116:119], v201 offset:8192
v_mfma_f32_16x16x32_bf16 v[100:103], a[108:111], v[132:135], v[100:103]
buffer_load_dwordx4 v218, s[4:7], s64 offen offset:1024 lds
s_waitcnt lgkmcnt(6)
v_mfma_f32_16x16x32_bf16 v[0:3], a[112:115], v[136:139], v[0:3]
ds_read_b128 v[104:107], v202 offset:8192
s_waitcnt lgkmcnt(4)
v_mfma_f32_16x16x32_bf16 v[52:55], a[112:115], v[140:143], v[52:55]
v_mfma_f32_16x16x32_bf16 v[4:7], v[116:119], v[136:139], v[4:7]
ds_read_b128 v[108:111], v203 offset:8192
v_mfma_f32_16x16x32_bf16 v[56:59], v[116:119], v[140:143], v[56:59]
buffer_load_dwordx4 v219, s[4:7], s64 offen offset:2048 lds
s_waitcnt lgkmcnt(4)
v_mfma_f32_16x16x32_bf16 v[8:11], v[120:123], v[136:139], v[8:11]
ds_read_b128 v[112:115], v204 offset:8192
v_mfma_f32_16x16x32_bf16 v[60:63], v[120:123], v[140:143], v[60:63]
s_waitcnt lgkmcnt(4)
v_mfma_f32_16x16x32_bf16 v[12:15], v[124:127], v[136:139], v[12:15]
ds_read_b128 a[120:123], v201 offset:16384
v_mfma_f32_16x16x32_bf16 v[64:67], v[124:127], v[140:143], v[64:67]
buffer_load_dwordx4 v220, s[4:7], s64 offen offset:3072 lds
s_waitcnt lgkmcnt(4)
v_mfma_f32_16x16x32_bf16 v[16:19], a[116:119], v[136:139], v[16:19]
ds_read_b128 v[116:119], v202 offset:16384
v_mfma_f32_16x16x32_bf16 v[68:71], a[116:119], v[140:143], v[68:71]
s_waitcnt lgkmcnt(4)
v_mfma_f32_16x16x32_bf16 v[20:23], v[104:107], v[136:139], v[20:23]
ds_read_b128 v[120:123], v203 offset:16384
v_mfma_f32_16x16x32_bf16 v[72:75], v[104:107], v[140:143], v[72:75]
s_add_u32 m0, s65, 110592
s_nop 0
buffer_load_dwordx4 v221, s[4:7], s64 offen offset:0 lds
s_waitcnt lgkmcnt(4)
v_mfma_f32_16x16x32_bf16 v[24:27], v[108:111], v[136:139], v[24:27]
ds_read_b128 v[124:127], v204 offset:16384
v_mfma_f32_16x16x32_bf16 v[76:79], v[108:111], v[140:143], v[76:79]
s_waitcnt lgkmcnt(4)
v_mfma_f32_16x16x32_bf16 v[28:31], v[112:115], v[136:139], v[28:31]
ds_read_b128 a[124:127], v205 offset:24576
v_mfma_f32_16x16x32_bf16 v[80:83], v[112:115], v[140:143], v[80:83]
buffer_load_dwordx4 v222, s[4:7], s64 offen offset:1024 lds
s_waitcnt lgkmcnt(4)
v_mfma_f32_16x16x32_bf16 v[32:35], a[120:123], v[136:139], v[32:35]
s_waitcnt vmcnt(10)
s_barrier
ds_read_b128 a[128:131], v196 offset:28672
v_mfma_f32_16x16x32_bf16 v[84:87], a[120:123], v[140:143], v[84:87]
ds_read_b64_tr_b16 v[128:129], v216 offset:16384
ds_read_b64_tr_b16 v[130:131], v216 offset:16896
s_waitcnt lgkmcnt(6)
v_mfma_f32_16x16x32_bf16 v[36:39], v[116:119], v[136:139], v[36:39]
ds_read_b128 v[104:107], v197 offset:28672
v_mfma_f32_16x16x32_bf16 v[88:91], v[116:119], v[140:143], v[88:91]
ds_read_b64_tr_b16 v[132:133], v216 offset:24576
ds_read_b64_tr_b16 v[134:135], v216 offset:25088
s_waitcnt lgkmcnt(8)
v_mfma_f32_16x16x32_bf16 v[40:43], v[120:123], v[136:139], v[40:43]
ds_read_b128 v[108:111], v198 offset:28672
v_mfma_f32_16x16x32_bf16 v[92:95], v[120:123], v[140:143], v[92:95]
s_waitcnt lgkmcnt(8)
v_mfma_f32_16x16x32_bf16 v[44:47], v[124:127], v[136:139], v[44:47]
ds_read_b128 v[112:115], v199 offset:28672
v_mfma_f32_16x16x32_bf16 v[96:99], v[124:127], v[140:143], v[96:99]
buffer_load_dwordx4 v223, s[4:7], s64 offen offset:2048 lds
s_waitcnt lgkmcnt(8)
v_mfma_f32_16x16x32_bf16 v[48:51], a[124:127], v[136:139], v[48:51]
ds_read_b128 a[132:135], v196 offset:36864
v_mfma_f32_16x16x32_bf16 v[100:103], a[124:127], v[140:143], v[100:103]
s_barrier
s_waitcnt lgkmcnt(6)
v_mfma_f32_16x16x32_bf16 v[0:3], a[128:131], v[128:131], v[0:3]
ds_read_b128 v[116:119], v197 offset:36864
s_waitcnt lgkmcnt(4)
v_mfma_f32_16x16x32_bf16 v[52:55], a[128:131], v[132:135], v[52:55]
s_add_u32 m0, s66, 57344
s_add_u32 s49, s49, 196608
s_cmp_eq_u32 s49, s1
s_cselect_b32 s49, s0, s49
buffer_load_dwordx4 v224, s[20:23], s49 offen offset:0 lds
v_mfma_f32_16x16x32_bf16 v[4:7], v[104:107], v[128:131], v[4:7]
ds_read_b128 v[120:123], v198 offset:36864
v_mfma_f32_16x16x32_bf16 v[56:59], v[104:107], v[132:135], v[56:59]
s_waitcnt lgkmcnt(4)
v_mfma_f32_16x16x32_bf16 v[8:11], v[108:111], v[128:131], v[8:11]
ds_read_b128 v[124:127], v199 offset:36864
v_mfma_f32_16x16x32_bf16 v[60:63], v[108:111], v[132:135], v[60:63]
buffer_load_dwordx4 v225, s[20:23], s49 offen offset:1024 lds
s_waitcnt lgkmcnt(4)
v_mfma_f32_16x16x32_bf16 v[12:15], v[112:115], v[128:131], v[12:15]
ds_read_b128 a[136:139], v196 offset:45056
v_mfma_f32_16x16x32_bf16 v[64:67], v[112:115], v[132:135], v[64:67]
s_waitcnt lgkmcnt(4)
v_mfma_f32_16x16x32_bf16 v[16:19], a[132:135], v[128:131], v[16:19]
ds_read_b128 v[104:107], v197 offset:45056
v_mfma_f32_16x16x32_bf16 v[68:71], a[132:135], v[132:135], v[68:71]
buffer_load_dwordx4 v226, s[20:23], s49 offen offset:2048 lds
s_waitcnt lgkmcnt(4)
v_mfma_f32_16x16x32_bf16 v[20:23], v[116:119], v[128:131], v[20:23]
ds_read_b128 v[108:111], v198 offset:45056
v_mfma_f32_16x16x32_bf16 v[72:75], v[116:119], v[132:135], v[72:75]
s_waitcnt lgkmcnt(4)
v_mfma_f32_16x16x32_bf16 v[24:27], v[120:123], v[128:131], v[24:27]
ds_read_b128 v[112:115], v199 offset:45056
v_mfma_f32_16x16x32_bf16 v[76:79], v[120:123], v[132:135], v[76:79]
buffer_load_dwordx4 v227, s[20:23], s49 offen offset:3072 lds
s_waitcnt lgkmcnt(4)
v_mfma_f32_16x16x32_bf16 v[28:31], v[124:127], v[128:131], v[28:31]
ds_read_b128 a[140:143], v200 offset:53248
v_mfma_f32_16x16x32_bf16 v[80:83], v[124:127], v[132:135], v[80:83]
s_waitcnt lgkmcnt(4)
v_mfma_f32_16x16x32_bf16 v[32:35], a[136:139], v[128:131], v[32:35]
ds_read_b128 a[144:147], v201 offset:28672
v_mfma_f32_16x16x32_bf16 v[84:87], a[136:139], v[132:135], v[84:87]
ds_read_b64_tr_b16 v[136:137], v216 offset:20480
ds_read_b64_tr_b16 v[138:139], v216 offset:20992
s_waitcnt lgkmcnt(6)
v_mfma_f32_16x16x32_bf16 v[36:39], v[104:107], v[128:131], v[36:39]
ds_read_b128 v[116:119], v202 offset:28672
v_mfma_f32_16x16x32_bf16 v[88:91], v[104:107], v[132:135], v[88:91]
ds_read_b64_tr_b16 v[140:141], v216 offset:28672
ds_read_b64_tr_b16 v[142:143], v216 offset:29184
s_waitcnt lgkmcnt(8)
v_mfma_f32_16x16x32_bf16 v[40:43], v[108:111], v[128:131], v[40:43]
ds_read_b128 v[120:123], v203 offset:28672
v_mfma_f32_16x16x32_bf16 v[92:95], v[108:111], v[132:135], v[92:95]
s_add_u32 m0, s65, 0
s_add_u32 s64, s64, 128
s_cmp_eq_u32 s64, s68
s_cselect_b32 s64, s59, s64
buffer_load_dwordx4 v217, s[4:7], s64 offen offset:0 lds
s_waitcnt lgkmcnt(8)
v_mfma_f32_16x16x32_bf16 v[44:47], v[112:115], v[128:131], v[44:47]
ds_read_b128 v[124:127], v204 offset:28672
v_mfma_f32_16x16x32_bf16 v[96:99], v[112:115], v[132:135], v[96:99]
s_waitcnt lgkmcnt(8)
v_mfma_f32_16x16x32_bf16 v[48:51], a[140:143], v[128:131], v[48:51]
ds_read_b128 a[148:151], v201 offset:36864
v_mfma_f32_16x16x32_bf16 v[100:103], a[140:143], v[132:135], v[100:103]
buffer_load_dwordx4 v218, s[4:7], s64 offen offset:1024 lds
s_waitcnt lgkmcnt(6)
v_mfma_f32_16x16x32_bf16 v[0:3], a[144:147], v[136:139], v[0:3]
ds_read_b128 v[104:107], v202 offset:36864
s_waitcnt lgkmcnt(4)
v_mfma_f32_16x16x32_bf16 v[52:55], a[144:147], v[140:143], v[52:55]
v_mfma_f32_16x16x32_bf16 v[4:7], v[116:119], v[136:139], v[4:7]
ds_read_b128 v[108:111], v203 offset:36864
v_mfma_f32_16x16x32_bf16 v[56:59], v[116:119], v[140:143], v[56:59]
buffer_load_dwordx4 v219, s[4:7], s64 offen offset:2048 lds
s_waitcnt lgkmcnt(4)
v_mfma_f32_16x16x32_bf16 v[8:11], v[120:123], v[136:139], v[8:11]
ds_read_b128 v[112:115], v204 offset:36864
v_mfma_f32_16x16x32_bf16 v[60:63], v[120:123], v[140:143], v[60:63]
s_waitcnt lgkmcnt(4)
v_mfma_f32_16x16x32_bf16 v[12:15], v[124:127], v[136:139], v[12:15]
ds_read_b128 a[152:155], v201 offset:45056
v_mfma_f32_16x16x32_bf16 v[64:67], v[124:127], v[140:143], v[64:67]
buffer_load_dwordx4 v220, s[4:7], s64 offen offset:3072 lds
s_waitcnt lgkmcnt(4)
v_mfma_f32_16x16x32_bf16 v[16:19], a[148:151], v[136:139], v[16:19]
ds_read_b128 v[116:119], v202 offset:45056
v_mfma_f32_16x16x32_bf16 v[68:71], a[148:151], v[140:143], v[68:71]
s_waitcnt lgkmcnt(4)
v_mfma_f32_16x16x32_bf16 v[20:23], v[104:107], v[136:139], v[20:23]
ds_read_b128 v[120:123], v203 offset:45056
v_mfma_f32_16x16x32_bf16 v[72:75], v[104:107], v[140:143], v[72:75]
s_add_u32 m0, s65, 4096
s_nop 0
buffer_load_dwordx4 v221, s[4:7], s64 offen offset:0 lds
s_waitcnt lgkmcnt(4)
v_mfma_f32_16x16x32_bf16 v[24:27], v[108:111], v[136:139], v[24:27]
ds_read_b128 v[124:127], v204 offset:45056
v_mfma_f32_16x16x32_bf16 v[76:79], v[108:111], v[140:143], v[76:79]
s_waitcnt lgkmcnt(4)
v_mfma_f32_16x16x32_bf16 v[28:31], v[112:115], v[136:139], v[28:31]
ds_read_b128 a[156:159], v205 offset:53248
v_mfma_f32_16x16x32_bf16 v[80:83], v[112:115], v[140:143], v[80:83]
buffer_load_dwordx4 v222, s[4:7], s64 offen offset:1024 lds
s_waitcnt lgkmcnt(4)
v_mfma_f32_16x16x32_bf16 v[32:35], a[152:155], v[136:139], v[32:35]
s_waitcnt vmcnt(10)
s_barrier
ds_read_b128 a[160:163], v206 offset:0
v_mfma_f32_16x16x32_bf16 v[84:87], a[152:155], v[140:143], v[84:87]
ds_read_b64_tr_b16 v[128:129], v216 offset:32768
ds_read_b64_tr_b16 v[130:131], v216 offset:33280
s_waitcnt lgkmcnt(6)
v_mfma_f32_16x16x32_bf16 v[36:39], v[116:119], v[136:139], v[36:39]
ds_read_b128 v[104:107], v207 offset:0
v_mfma_f32_16x16x32_bf16 v[88:91], v[116:119], v[140:143], v[88:91]
ds_read_b64_tr_b16 v[132:133], v216 offset:40960
ds_read_b64_tr_b16 v[134:135], v216 offset:41472
s_waitcnt lgkmcnt(8)
v_mfma_f32_16x16x32_bf16 v[40:43], v[120:123], v[136:139], v[40:43]
ds_read_b128 v[108:111], v208 offset:0
v_mfma_f32_16x16x32_bf16 v[92:95], v[120:123], v[140:143], v[92:95]
s_waitcnt lgkmcnt(8)
v_mfma_f32_16x16x32_bf16 v[44:47], v[124:127], v[136:139], v[44:47]
ds_read_b128 v[112:115], v209 offset:0
v_mfma_f32_16x16x32_bf16 v[96:99], v[124:127], v[140:143], v[96:99]
buffer_load_dwordx4 v223, s[4:7], s64 offen offset:2048 lds
s_waitcnt lgkmcnt(8)
v_mfma_f32_16x16x32_bf16 v[48:51], a[156:159], v[136:139], v[48:51]
ds_read_b128 a[164:167], v206 offset:8192
v_mfma_f32_16x16x32_bf16 v[100:103], a[156:159], v[140:143], v[100:103]
s_barrier
s_waitcnt lgkmcnt(6)
v_mfma_f32_16x16x32_bf16 v[0:3], a[160:163], v[128:131], v[0:3]
ds_read_b128 v[116:119], v207 offset:8192
s_waitcnt lgkmcnt(4)
v_mfma_f32_16x16x32_bf16 v[52:55], a[160:163], v[132:135], v[52:55]
s_add_u32 m0, s66, 73728
s_add_u32 s49, s49, 196608
s_cmp_eq_u32 s49, s1
s_cselect_b32 s49, s0, s49
buffer_load_dwordx4 v224, s[20:23], s49 offen offset:0 lds
v_mfma_f32_16x16x32_bf16 v[4:7], v[104:107], v[128:131], v[4:7]
ds_read_b128 v[120:123], v208 offset:8192
v_mfma_f32_16x16x32_bf16 v[56:59], v[104:107], v[132:135], v[56:59]
s_waitcnt lgkmcnt(4)
v_mfma_f32_16x16x32_bf16 v[8:11], v[108:111], v[128:131], v[8:11]
ds_read_b128 v[124:127], v209 offset:8192
v_mfma_f32_16x16x32_bf16 v[60:63], v[108:111], v[132:135], v[60:63]
buffer_load_dwordx4 v225, s[20:23], s49 offen offset:1024 lds
s_waitcnt lgkmcnt(4)
v_mfma_f32_16x16x32_bf16 v[12:15], v[112:115], v[128:131], v[12:15]
ds_read_b128 a[168:171], v206 offset:16384
v_mfma_f32_16x16x32_bf16 v[64:67], v[112:115], v[132:135], v[64:67]
s_waitcnt lgkmcnt(4)
v_mfma_f32_16x16x32_bf16 v[16:19], a[164:167], v[128:131], v[16:19]
ds_read_b128 v[104:107], v207 offset:16384
v_mfma_f32_16x16x32_bf16 v[68:71], a[164:167], v[132:135], v[68:71]
buffer_load_dwordx4 v226, s[20:23], s49 offen offset:2048 lds
s_waitcnt lgkmcnt(4)
v_mfma_f32_16x16x32_bf16 v[20:23], v[116:119], v[128:131], v[20:23]
ds_read_b128 v[108:111], v208 offset:16384
v_mfma_f32_16x16x32_bf16 v[72:75], v[116:119], v[132:135], v[72:75]
s_waitcnt lgkmcnt(4)
v_mfma_f32_16x16x32_bf16 v[24:27], v[120:123], v[128:131], v[24:27]
ds_read_b128 v[112:115], v209 offset:16384
v_mfma_f32_16x16x32_bf16 v[76:79], v[120:123], v[132:135], v[76:79]
buffer_load_dwordx4 v227, s[20:23], s49 offen offset:3072 lds
s_waitcnt lgkmcnt(4)
v_mfma_f32_16x16x32_bf16 v[28:31], v[124:127], v[128:131], v[28:31]
ds_read_b128 a[172:175], v210 offset:24576
v_mfma_f32_16x16x32_bf16 v[80:83], v[124:127], v[132:135], v[80:83]
s_waitcnt lgkmcnt(4)
v_mfma_f32_16x16x32_bf16 v[32:35], a[168:171], v[128:131], v[32:35]
ds_read_b128 a[176:179], v211 offset:0
v_mfma_f32_16x16x32_bf16 v[84:87], a[168:171], v[132:135], v[84:87]
ds_read_b64_tr_b16 v[136:137], v216 offset:36864
ds_read_b64_tr_b16 v[138:139], v216 offset:37376
s_waitcnt lgkmcnt(6)
v_mfma_f32_16x16x32_bf16 v[36:39], v[104:107], v[128:131], v[36:39]
ds_read_b128 v[116:119], v212 offset:0
v_mfma_f32_16x16x32_bf16 v[88:91], v[104:107], v[132:135], v[88:91]
ds_read_b64_tr_b16 v[140:141], v216 offset:45056
ds_read_b64_tr_b16 v[142:143], v216 offset:45568
s_waitcnt lgkmcnt(8)
v_mfma_f32_16x16x32_bf16 v[40:43], v[108:111], v[128:131], v[40:43]
ds_read_b128 v[120:123], v213 offset:0
v_mfma_f32_16x16x32_bf16 v[92:95], v[108:111], v[132:135], v[92:95]
s_add_u32 m0, s65, 28672
s_add_u32 s64, s64, 128
s_cmp_eq_u32 s64, s68
s_cselect_b32 s64, s59, s64
buffer_load_dwordx4 v217, s[4:7], s64 offen offset:0 lds
s_waitcnt lgkmcnt(8)
v_mfma_f32_16x16x32_bf16 v[44:47], v[112:115], v[128:131], v[44:47]
ds_read_b128 v[124:127], v214 offset:0
v_mfma_f32_16x16x32_bf16 v[96:99], v[112:115], v[132:135], v[96:99]
s_waitcnt lgkmcnt(8)
v_mfma_f32_16x16x32_bf16 v[48:51], a[172:175], v[128:131], v[48:51]
ds_read_b128 a[180:183], v211 offset:8192
v_mfma_f32_16x16x32_bf16 v[100:103], a[172:175], v[132:135], v[100:103]
buffer_load_dwordx4 v218, s[4:7], s64 offen offset:1024 lds
s_waitcnt lgkmcnt(6)
v_mfma_f32_16x16x32_bf16 v[0:3], a[176:179], v[136:139], v[0:3]
ds_read_b128 v[104:107], v212 offset:8192
s_waitcnt lgkmcnt(4)
v_mfma_f32_16x16x32_bf16 v[52:55], a[176:179], v[140:143], v[52:55]
v_mfma_f32_16x16x32_bf16 v[4:7], v[116:119], v[136:139], v[4:7]
ds_read_b128 v[108:111], v213 offset:8192
v_mfma_f32_16x16x32_bf16 v[56:59], v[116:119], v[140:143], v[56:59]
buffer_load_dwordx4 v219, s[4:7], s64 offen offset:2048 lds
s_waitcnt lgkmcnt(4)
v_mfma_f32_16x16x32_bf16 v[8:11], v[120:123], v[136:139], v[8:11]
ds_read_b128 v[112:115], v214 offset:8192
v_mfma_f32_16x16x32_bf16 v[60:63], v[120:123], v[140:143], v[60:63]
s_waitcnt lgkmcnt(4)
v_mfma_f32_16x16x32_bf16 v[12:15], v[124:127], v[136:139], v[12:15]
ds_read_b128 a[184:187], v211 offset:16384
v_mfma_f32_16x16x32_bf16 v[64:67], v[124:127], v[140:143], v[64:67]
buffer_load_dwordx4 v220, s[4:7], s64 offen offset:3072 lds
s_waitcnt lgkmcnt(4)
v_mfma_f32_16x16x32_bf16 v[16:19], a[180:183], v[136:139], v[16:19]
ds_read_b128 v[116:119], v212 offset:16384
v_mfma_f32_16x16x32_bf16 v[68:71], a[180:183], v[140:143], v[68:71]
s_waitcnt lgkmcnt(4)
v_mfma_f32_16x16x32_bf16 v[20:23], v[104:107], v[136:139], v[20:23]
ds_read_b128 v[120:123], v213 offset:16384
v_mfma_f32_16x16x32_bf16 v[72:75], v[104:107], v[140:143], v[72:75]
s_add_u32 m0, s65, 32768
s_nop 0
buffer_load_dwordx4 v221, s[4:7], s64 offen offset:0 lds
s_waitcnt lgkmcnt(4)
v_mfma_f32_16x16x32_bf16 v[24:27], v[108:111], v[136:139], v[24:27]
ds_read_b128 v[124:127], v214 offset:16384
v_mfma_f32_16x16x32_bf16 v[76:79], v[108:111], v[140:143], v[76:79]
s_waitcnt lgkmcnt(4)
v_mfma_f32_16x16x32_bf16 v[28:31], v[112:115], v[136:139], v[28:31]
ds_read_b128 a[188:191], v215 offset:24576
v_mfma_f32_16x16x32_bf16 v[80:83], v[112:115], v[140:143], v[80:83]
buffer_load_dwordx4 v222, s[4:7], s64 offen offset:1024 lds
buffer_load_dwordx2 v[250:251], v249, s[12:15], 0 offen sc1
s_waitcnt lgkmcnt(4)
v_mfma_f32_16x16x32_bf16 v[32:35], a[184:187], v[136:139], v[32:35]
s_waitcnt vmcnt(11)
s_barrier
ds_read_b128 a[192:195], v196 offset:0
v_mfma_f32_16x16x32_bf16 v[84:87], a[184:187], v[140:143], v[84:87]
ds_read_b64_tr_b16 v[128:129], v216 offset:0
ds_read_b64_tr_b16 v[130:131], v216 offset:512
s_waitcnt lgkmcnt(6)
v_mfma_f32_16x16x32_bf16 v[36:39], v[116:119], v[136:139], v[36:39]
ds_read_b128 v[104:107], v197 offset:0
v_mfma_f32_16x16x32_bf16 v[88:91], v[116:119], v[140:143], v[88:91]
ds_read_b64_tr_b16 v[132:133], v216 offset:8192
ds_read_b64_tr_b16 v[134:135], v216 offset:8704
s_waitcnt lgkmcnt(8)
v_mfma_f32_16x16x32_bf16 v[40:43], v[120:123], v[136:139], v[40:43]
ds_read_b128 v[108:111], v198 offset:0
v_mfma_f32_16x16x32_bf16 v[92:95], v[120:123], v[140:143], v[92:95]
s_waitcnt lgkmcnt(8)
v_mfma_f32_16x16x32_bf16 v[44:47], v[124:127], v[136:139], v[44:47]
ds_read_b128 v[112:115], v199 offset:0
v_mfma_f32_16x16x32_bf16 v[96:99], v[124:127], v[140:143], v[96:99]
buffer_load_dwordx4 v223, s[4:7], s64 offen offset:2048 lds
s_waitcnt lgkmcnt(8)
v_mfma_f32_16x16x32_bf16 v[48:51], a[188:191], v[136:139], v[48:51]
ds_read_b128 a[196:199], v196 offset:8192
v_mfma_f32_16x16x32_bf16 v[100:103], a[188:191], v[140:143], v[100:103]
s_barrier
s_waitcnt lgkmcnt(6)
v_mfma_f32_16x16x32_bf16 v[0:3], a[192:195], v[128:131], v[0:3]
ds_read_b128 v[116:119], v197 offset:8192
s_waitcnt lgkmcnt(4)
v_mfma_f32_16x16x32_bf16 v[52:55], a[192:195], v[132:135], v[52:55]
s_lshl_b32 s66, s69, 13
s_add_u32 m0, s66, 98304
s_nop 0
buffer_load_dwordx4 v224, s[20:23], s50 offen offset:0 lds
v_mfma_f32_16x16x32_bf16 v[4:7], v[104:107], v[128:131], v[4:7]
ds_read_b128 v[120:123], v198 offset:8192
v_mfma_f32_16x16x32_bf16 v[56:59], v[104:107], v[132:135], v[56:59]
s_waitcnt lgkmcnt(4)
v_mfma_f32_16x16x32_bf16 v[8:11], v[108:111], v[128:131], v[8:11]
ds_read_b128 v[124:127], v199 offset:8192
v_mfma_f32_16x16x32_bf16 v[60:63], v[108:111], v[132:135], v[60:63]
buffer_load_dwordx4 v225, s[20:23], s50 offen offset:1024 lds
s_waitcnt lgkmcnt(4)
v_mfma_f32_16x16x32_bf16 v[12:15], v[112:115], v[128:131], v[12:15]
ds_read_b128 a[200:203], v196 offset:16384
v_mfma_f32_16x16x32_bf16 v[64:67], v[112:115], v[132:135], v[64:67]
s_waitcnt lgkmcnt(4)
v_mfma_f32_16x16x32_bf16 v[16:19], a[196:199], v[128:131], v[16:19]
ds_read_b128 v[104:107], v197 offset:16384
v_mfma_f32_16x16x32_bf16 v[68:71], a[196:199], v[132:135], v[68:71]
buffer_load_dwordx4 v226, s[20:23], s50 offen offset:2048 lds
s_waitcnt lgkmcnt(4)
v_mfma_f32_16x16x32_bf16 v[20:23], v[116:119], v[128:131], v[20:23]
ds_read_b128 v[108:111], v198 offset:16384
v_mfma_f32_16x16x32_bf16 v[72:75], v[116:119], v[132:135], v[72:75]
s_waitcnt lgkmcnt(4)
v_mfma_f32_16x16x32_bf16 v[24:27], v[120:123], v[128:131], v[24:27]
ds_read_b128 v[112:115], v199 offset:16384
v_mfma_f32_16x16x32_bf16 v[76:79], v[120:123], v[132:135], v[76:79]
buffer_load_dwordx4 v227, s[20:23], s50 offen offset:3072 lds
s_waitcnt lgkmcnt(4)
v_mfma_f32_16x16x32_bf16 v[28:31], v[124:127], v[128:131], v[28:31]
ds_read_b128 a[204:207], v200 offset:24576
v_mfma_f32_16x16x32_bf16 v[80:83], v[124:127], v[132:135], v[80:83]
s_waitcnt lgkmcnt(4)
v_mfma_f32_16x16x32_bf16 v[32:35], a[200:203], v[128:131], v[32:35]
ds_read_b128 a[208:211], v201 offset:0
v_mfma_f32_16x16x32_bf16 v[84:87], a[200:203], v[132:135], v[84:87]
ds_read_b64_tr_b16 v[136:137], v216 offset:4096
ds_read_b64_tr_b16 v[138:139], v216 offset:4608
s_waitcnt lgkmcnt(6)
v_mfma_f32_16x16x32_bf16 v[36:39], v[104:107], v[128:131], v[36:39]
ds_read_b128 v[116:119], v202 offset:0
v_mfma_f32_16x16x32_bf16 v[88:91], v[104:107], v[132:135], v[88:91]
ds_read_b64_tr_b16 v[140:141], v216 offset:12288
ds_read_b64_tr_b16 v[142:143], v216 offset:12800
s_waitcnt lgkmcnt(8)
v_mfma_f32_16x16x32_bf16 v[40:43], v[108:111], v[128:131], v[40:43]
ds_read_b128 v[120:123], v203 offset:0
v_mfma_f32_16x16x32_bf16 v[92:95], v[108:111], v[132:135], v[92:95]
s_add_u32 m0, s66, 102400
s_add_u32 s50, s50, 98304
buffer_load_dwordx4 v224, s[20:23], s50 offen offset:0 lds
s_waitcnt lgkmcnt(8)
v_mfma_f32_16x16x32_bf16 v[44:47], v[112:115], v[128:131], v[44:47]
ds_read_b128 v[124:127], v204 offset:0
v_mfma_f32_16x16x32_bf16 v[96:99], v[112:115], v[132:135], v[96:99]
s_waitcnt lgkmcnt(8)
v_mfma_f32_16x16x32_bf16 v[48:51], a[204:207], v[128:131], v[48:51]
ds_read_b128 a[212:215], v201 offset:8192
v_mfma_f32_16x16x32_bf16 v[100:103], a[204:207], v[132:135], v[100:103]
buffer_load_dwordx4 v225, s[20:23], s50 offen offset:1024 lds
s_waitcnt lgkmcnt(6)
v_mfma_f32_16x16x32_bf16 v[0:3], a[208:211], v[136:139], v[0:3]
ds_read_b128 v[104:107], v202 offset:8192
s_waitcnt lgkmcnt(4)
v_mfma_f32_16x16x32_bf16 v[52:55], a[208:211], v[140:143], v[52:55]
v_mfma_f32_16x16x32_bf16 v[4:7], v[116:119], v[136:139], v[4:7]
ds_read_b128 v[108:111], v203 offset:8192
v_mfma_f32_16x16x32_bf16 v[56:59], v[116:119], v[140:143], v[56:59]
buffer_load_dwordx4 v226, s[20:23], s50 offen offset:2048 lds
s_waitcnt lgkmcnt(4)
v_mfma_f32_16x16x32_bf16 v[8:11], v[120:123], v[136:139], v[8:11]
ds_read_b128 v[112:115], v204 offset:8192
v_mfma_f32_16x16x32_bf16 v[60:63], v[120:123], v[140:143], v[60:63]
s_waitcnt lgkmcnt(4)
v_mfma_f32_16x16x32_bf16 v[12:15], v[124:127], v[136:139], v[12:15]
ds_read_b128 a[216:219], v201 offset:16384
v_mfma_f32_16x16x32_bf16 v[64:67], v[124:127], v[140:143], v[64:67]
buffer_load_dwordx4 v227, s[20:23], s50 offen offset:3072 lds
s_waitcnt lgkmcnt(4)
v_mfma_f32_16x16x32_bf16 v[16:19], a[212:215], v[136:139], v[16:19]
ds_read_b128 v[116:119], v202 offset:16384
v_mfma_f32_16x16x32_bf16 v[68:71], a[212:215], v[140:143], v[68:71]
s_waitcnt lgkmcnt(4)
v_mfma_f32_16x16x32_bf16 v[20:23], v[104:107], v[136:139], v[20:23]
ds_read_b128 v[120:123], v203 offset:16384
v_mfma_f32_16x16x32_bf16 v[72:75], v[104:107], v[140:143], v[72:75]
s_waitcnt lgkmcnt(4)
v_mfma_f32_16x16x32_bf16 v[24:27], v[108:111], v[136:139], v[24:27]
ds_read_b128 v[124:127], v204 offset:16384
v_mfma_f32_16x16x32_bf16 v[76:79], v[108:111], v[140:143], v[76:79]
s_waitcnt lgkmcnt(4)
v_mfma_f32_16x16x32_bf16 v[28:31], v[112:115], v[136:139], v[28:31]
ds_read_b128 a[220:223], v205 offset:24576
v_mfma_f32_16x16x32_bf16 v[80:83], v[112:115], v[140:143], v[80:83]
s_waitcnt lgkmcnt(4)
v_mfma_f32_16x16x32_bf16 v[32:35], a[216:219], v[136:139], v[32:35]
s_waitcnt vmcnt(8)
s_barrier
ds_read_b128 a[224:227], v196 offset:28672
v_mfma_f32_16x16x32_bf16 v[84:87], a[216:219], v[140:143], v[84:87]
ds_read_b64_tr_b16 v[128:129], v216 offset:16384
ds_read_b64_tr_b16 v[130:131], v216 offset:16896
s_waitcnt lgkmcnt(6)
v_mfma_f32_16x16x32_bf16 v[36:39], v[116:119], v[136:139], v[36:39]
ds_read_b128 v[104:107], v197 offset:28672
v_mfma_f32_16x16x32_bf16 v[88:91], v[116:119], v[140:143], v[88:91]
ds_read_b64_tr_b16 v[132:133], v216 offset:24576
ds_read_b64_tr_b16 v[134:135], v216 offset:25088
s_waitcnt lgkmcnt(8)
v_mfma_f32_16x16x32_bf16 v[40:43], v[120:123], v[136:139], v[40:43]
ds_read_b128 v[108:111], v198 offset:28672
v_mfma_f32_16x16x32_bf16 v[92:95], v[120:123], v[140:143], v[92:95]
s_waitcnt lgkmcnt(8)
v_mfma_f32_16x16x32_bf16 v[44:47], v[124:127], v[136:139], v[44:47]
ds_read_b128 v[112:115], v199 offset:28672
v_mfma_f32_16x16x32_bf16 v[96:99], v[124:127], v[140:143], v[96:99]
s_waitcnt lgkmcnt(8)
v_mfma_f32_16x16x32_bf16 v[48:51], a[220:223], v[136:139], v[48:51]
ds_read_b128 a[228:231], v196 offset:36864
v_mfma_f32_16x16x32_bf16 v[100:103], a[220:223], v[140:143], v[100:103]
s_barrier
s_waitcnt lgkmcnt(6)
v_mfma_f32_16x16x32_bf16 v[0:3], a[224:227], v[128:131], v[0:3]
ds_read_b128 v[116:119], v197 offset:36864
s_waitcnt lgkmcnt(4)
v_mfma_f32_16x16x32_bf16 v[52:55], a[224:227], v[132:135], v[52:55]
v_mfma_f32_16x16x32_bf16 v[4:7], v[104:107], v[128:131], v[4:7]
ds_read_b128 v[120:123], v198 offset:36864
v_mfma_f32_16x16x32_bf16 v[56:59], v[104:107], v[132:135], v[56:59]
s_waitcnt lgkmcnt(4)
v_mfma_f32_16x16x32_bf16 v[8:11], v[108:111], v[128:131], v[8:11]
ds_read_b128 v[124:127], v199 offset:36864
v_mfma_f32_16x16x32_bf16 v[60:63], v[108:111], v[132:135], v[60:63]
s_waitcnt lgkmcnt(4)
v_mfma_f32_16x16x32_bf16 v[12:15], v[112:115], v[128:131], v[12:15]
ds_read_b128 a[232:235], v196 offset:45056
v_mfma_f32_16x16x32_bf16 v[64:67], v[112:115], v[132:135], v[64:67]
s_waitcnt lgkmcnt(4)
v_mfma_f32_16x16x32_bf16 v[16:19], a[228:231], v[128:131], v[16:19]
ds_read_b128 v[104:107], v197 offset:45056
v_mfma_f32_16x16x32_bf16 v[68:71], a[228:231], v[132:135], v[68:71]
s_waitcnt lgkmcnt(4)
v_mfma_f32_16x16x32_bf16 v[20:23], v[116:119], v[128:131], v[20:23]
ds_read_b128 v[108:111], v198 offset:45056
v_mfma_f32_16x16x32_bf16 v[72:75], v[116:119], v[132:135], v[72:75]
s_waitcnt lgkmcnt(4)
v_mfma_f32_16x16x32_bf16 v[24:27], v[120:123], v[128:131], v[24:27]
ds_read_b128 v[112:115], v199 offset:45056
v_mfma_f32_16x16x32_bf16 v[76:79], v[120:123], v[132:135], v[76:79]
s_waitcnt lgkmcnt(4)
v_mfma_f32_16x16x32_bf16 v[28:31], v[124:127], v[128:131], v[28:31]
ds_read_b128 a[236:239], v200 offset:53248
v_mfma_f32_16x16x32_bf16 v[80:83], v[124:127], v[132:135], v[80:83]
s_waitcnt lgkmcnt(4)
v_mfma_f32_16x16x32_bf16 v[32:35], a[232:235], v[128:131], v[32:35]
ds_read_b128 a[240:243], v201 offset:28672
v_mfma_f32_16x16x32_bf16 v[84:87], a[232:235], v[132:135], v[84:87]
ds_read_b64_tr_b16 v[136:137], v216 offset:20480
ds_read_b64_tr_b16 v[138:139], v216 offset:20992
s_waitcnt lgkmcnt(6)
v_mfma_f32_16x16x32_bf16 v[36:39], v[104:107], v[128:131], v[36:39]
ds_read_b128 v[116:119], v202 offset:28672
v_mfma_f32_16x16x32_bf16 v[88:91], v[104:107], v[132:135], v[88:91]
ds_read_b64_tr_b16 v[140:141], v216 offset:28672
ds_read_b64_tr_b16 v[142:143], v216 offset:29184
v_cmp_ne_u64 vcc, s[46:47], v[250:251]
s_mov_b32 s54, 0
s_nop 0
s_cbranch_vccz POLLD_4
POLL_3:
buffer_load_dwordx2 v[250:251], v249, s[12:15], 0 offen sc1
s_waitcnt vmcnt(0)
v_cmp_ne_u64 vcc, s[46:47], v[250:251]
s_nop 1
s_cbranch_vccz POLLD_4
s_add_u32 s54, s54, 1
s_cmp_lt_u32 s54, 200000
s_sleep 1
s_cbranch_scc1 POLL_3
POLLD_4:
buffer_load_dword v196, v245, s[12:15], s67 offen sc1
s_waitcnt lgkmcnt(8)
v_mfma_f32_16x16x32_bf16 v[40:43], v[108:111], v[128:131], v[40:43]
ds_read_b128 v[120:123], v203 offset:28672
v_mfma_f32_16x16x32_bf16 v[92:95], v[108:111], v[132:135], v[92:95]
s_add_u32 m0, s66, 131072
s_mov_b32 s50, s60
buffer_load_dwordx4 v224, s[20:23], s50 offen offset:0 lds
buffer_load_dword v197, v246, s[12:15], s67 offen sc1
s_waitcnt lgkmcnt(8)
v_mfma_f32_16x16x32_bf16 v[44:47], v[112:115], v[128:131], v[44:47]
ds_read_b128 v[124:127], v204 offset:28672
v_mfma_f32_16x16x32_bf16 v[96:99], v[112:115], v[132:135], v[96:99]
buffer_load_dword v198, v252, s[12:15], s67 offen sc1
s_waitcnt lgkmcnt(8)
v_mfma_f32_16x16x32_bf16 v[48:51], a[236:239], v[128:131], v[48:51]
ds_read_b128 a[244:247], v201 offset:36864
v_mfma_f32_16x16x32_bf16 v[100:103], a[236:239], v[132:135], v[100:103]
buffer_load_dwordx4 v225, s[20:23], s50 offen offset:1024 lds
buffer_load_dword v199, v253, s[12:15], s67 offen sc1
s_waitcnt lgkmcnt(6)
v_mfma_f32_16x16x32_bf16 v[0:3], a[240:243], v[136:139], v[0:3]
ds_read_b128 v[104:107], v202 offset:36864
s_waitcnt lgkmcnt(4)
v_mfma_f32_16x16x32_bf16 v[52:55], a[240:243], v[140:143], v[52:55]
v_mfma_f32_16x16x32_bf16 v[4:7], v[116:119], v[136:139], v[4:7]
ds_read_b128 v[108:111], v203 offset:36864
v_mfma_f32_16x16x32_bf16 v[56:59], v[116:119], v[140:143], v[56:59]
buffer_load_dwordx4 v226, s[20:23], s50 offen offset:2048 lds
s_waitcnt lgkmcnt(4)
v_mfma_f32_16x16x32_bf16 v[8:11], v[120:123], v[136:139], v[8:11]
ds_read_b128 v[112:115], v204 offset:36864
v_mfma_f32_16x16x32_bf16 v[60:63], v[120:123], v[140:143], v[60:63]
s_waitcnt lgkmcnt(4)
v_mfma_f32_16x16x32_bf16 v[12:15], v[124:127], v[136:139], v[12:15]
ds_read_b128 a[248:251], v201 offset:45056
v_mfma_f32_16x16x32_bf16 v[64:67], v[124:127], v[140:143], v[64:67]
buffer_load_dwordx4 v227, s[20:23], s50 offen offset:3072 lds
s_waitcnt lgkmcnt(4)
v_mfma_f32_16x16x32_bf16 v[16:19], a[244:247], v[136:139], v[16:19]
ds_read_b128 v[116:119], v202 offset:45056
v_mfma_f32_16x16x32_bf16 v[68:71], a[244:247], v[140:143], v[68:71]
s_waitcnt lgkmcnt(4)
v_mfma_f32_16x16x32_bf16 v[20:23], v[104:107], v[136:139], v[20:23]
ds_read_b128 v[120:123], v203 offset:45056
v_mfma_f32_16x16x32_bf16 v[72:75], v[104:107], v[140:143], v[72:75]
s_add_u32 m0, s66, 135168
s_add_u32 s50, s50, 98304
buffer_load_dwordx4 v224, s[20:23], s50 offen offset:0 lds
s_waitcnt lgkmcnt(4)
v_mfma_f32_16x16x32_bf16 v[24:27], v[108:111], v[136:139], v[24:27]
ds_read_b128 v[124:127], v204 offset:45056
v_mfma_f32_16x16x32_bf16 v[76:79], v[108:111], v[140:143], v[76:79]
s_waitcnt lgkmcnt(4)
v_mfma_f32_16x16x32_bf16 v[28:31], v[112:115], v[136:139], v[28:31]
ds_read_b128 a[252:255], v205 offset:53248
v_mfma_f32_16x16x32_bf16 v[80:83], v[112:115], v[140:143], v[80:83]
buffer_load_dwordx4 v225, s[20:23], s50 offen offset:1024 lds
s_waitcnt lgkmcnt(4)
v_mfma_f32_16x16x32_bf16 v[32:35], a[248:251], v[136:139], v[32:35]
v_mfma_f32_16x16x32_bf16 v[84:87], a[248:251], v[140:143], v[84:87]
s_waitcnt lgkmcnt(3)
v_mfma_f32_16x16x32_bf16 v[36:39], v[116:119], v[136:139], v[36:39]
v_mfma_f32_16x16x32_bf16 v[88:91], v[116:119], v[140:143], v[88:91]
buffer_load_dwordx4 v226, s[20:23], s50 offen offset:2048 lds
s_waitcnt lgkmcnt(2)
v_mfma_f32_16x16x32_bf16 v[40:43], v[120:123], v[136:139], v[40:43]
v_mfma_f32_16x16x32_bf16 v[92:95], v[120:123], v[140:143], v[92:95]
s_waitcnt lgkmcnt(1)
v_mfma_f32_16x16x32_bf16 v[44:47], v[124:127], v[136:139], v[44:47]
v_mfma_f32_16x16x32_bf16 v[96:99], v[124:127], v[140:143], v[96:99]
buffer_load_dwordx4 v227, s[20:23], s50 offen offset:3072 lds
s_waitcnt lgkmcnt(0)
v_mfma_f32_16x16x32_bf16 v[48:51], a[252:255], v[136:139], v[48:51]
v_mfma_f32_16x16x32_bf16 v[100:103], a[252:255], v[140:143], v[100:103]
s_mov_b32 s60, 0x3fb8aa3b
s_mov_b32 s61, 0xbfb8aa3b
s_mov_b32 s62, 0x358637bd
s_waitcnt vmcnt(12)
s_barrier
v_pk_fma_f32 v[160:161], v[0:1], s[60:61], s[60:61] op_sel:[0,0,1] op_sel_hi:[1,0,1]
v_pk_fma_f32 v[162:163], v[2:3], s[60:61], s[60:61] op_sel:[0,0,1] op_sel_hi:[1,0,1]
v_pk_fma_f32 v[164:165], v[4:5], s[60:61], s[60:61] op_sel:[0,0,1] op_sel_hi:[1,0,1]
v_pk_fma_f32 v[166:167], v[6:7], s[60:61], s[60:61] op_sel:[0,0,1] op_sel_hi:[1,0,1]
v_exp_f32_e64 v160, v160 clamp
s_mul_i32 s49, s69, 1441792
s_lshl_b32 s51, s57, 7
v_exp_f32_e64 v161, v161 clamp
s_add_u32 s49, s49, s51
s_lshl_b32 s50, s69, 17
v_exp_f32_e64 v162, v162 clamp
s_lshl_b32 s51, s58, 7
s_add_u32 s50, s50, s51
v_exp_f32_e64 v163, v163 clamp
s_mov_b32 s64, s49
s_add_u32 s65, s49, 1441792
v_exp_f32_e64 v164, v164 clamp
s_bfe_u32 s51, s45, 0x40004
s_mul_i32 s51, s51, 131072
v_exp_f32_e64 v165, v165 clamp
s_add_u32 s49, s49, s51
s_mov_b32 s0, s50
v_exp_f32_e64 v166, v166 clamp
s_add_u32 s1, s50, 131072
s_bfe_u32 s51, s45, 0x20008
v_exp_f32_e64 v167, v167 clamp
s_mul_i32 s51, s51, 32768
s_add_u32 s50, s50, s51
v_max_f32 v160, v160, v0
v_lshrrev_b32 v244, 3, v240
v_mul_u32_u24 v244, 4096, v244
v_add_u32 v244, v244, v243
s_add_u32 m0, s48, 0
s_nop 0
buffer_load_dwordx4 v244, s[24:27], s49 offen offset:0 lds
v_max_f32 v161, v161, v1
v_max_f32 v162, v162, v2
v_max_f32 v163, v163, v3
s_add_u32 s49, s49, 31744
buffer_load_dwordx4 v244, s[24:27], s49 offen offset:1024 lds
v_max_f32 v164, v164, v4
v_max_f32 v165, v165, v5
v_max_f32 v166, v166, v6
s_add_u32 s49, s49, 31744
buffer_load_dwordx4 v244, s[24:27], s49 offen offset:2048 lds
v_max_f32 v167, v167, v7
v_pk_add_f32 v[160:161], v[160:161], s[62:63] op_sel_hi:[1,0]
v_pk_add_f32 v[162:163], v[162:163], s[62:63] op_sel_hi:[1,0]
s_add_u32 s49, s49, 31744
buffer_load_dwordx4 v244, s[24:27], s49 offen offset:3072 lds
v_pk_add_f32 v[164:165], v[164:165], s[62:63] op_sel_hi:[1,0]
v_pk_add_f32 v[166:167], v[166:167], s[62:63] op_sel_hi:[1,0]
v_cvt_pk_bf16_f32 v0, v160, v161
s_add_u32 m0, s48, 4096
s_add_u32 s49, s49, 35840
s_cmp_eq_u32 s49, s65
s_cselect_b32 s49, s64, s49
buffer_load_dwordx4 v244, s[24:27], s49 offen offset:0 lds
v_cvt_pk_bf16_f32 v1, v162, v163
v_cvt_pk_bf16_f32 v2, v164, v165
v_cvt_pk_bf16_f32 v3, v166, v167
s_add_u32 s49, s49, 31744
buffer_load_dwordx4 v244, s[24:27], s49 offen offset:1024 lds
v_cvt_pk_bf16_f32 v52, v52, v53
v_cvt_pk_bf16_f32 v53, v54, v55
v_cvt_pk_bf16_f32 v54, v56, v57
v_cvt_pk_bf16_f32 v55, v58, v59
v_mfma_f32_16x16x32_bf16 v[188:191], v[0:3], v[144:147], 0
s_nop 0
v_mfma_f32_16x16x32_bf16 v[184:187], v[0:3], v[52:55], 0
v_pk_fma_f32 v[160:161], v[8:9], s[60:61], s[60:61] op_sel:[0,0,1] op_sel_hi:[1,0,1]
v_pk_fma_f32 v[162:163], v[10:11], s[60:61], s[60:61] op_sel:[0,0,1] op_sel_hi:[1,0,1]
v_pk_fma_f32 v[164:165], v[12:13], s[60:61], s[60:61] op_sel:[0,0,1] op_sel_hi:[1,0,1]
s_add_u32 s49, s49, 31744
buffer_load_dwordx4 v244, s[24:27], s49 offen offset:2048 lds
v_pk_fma_f32 v[166:167], v[14:15], s[60:61], s[60:61] op_sel:[0,0,1] op_sel_hi:[1,0,1]
v_exp_f32_e64 v160, v160 clamp
ds_read_b64_tr_b16 v[104:105], v228 offset:0
v_exp_f32_e64 v161, v161 clamp
ds_read_b64_tr_b16 v[106:107], v228 offset:512
s_add_u32 s49, s49, 31744
buffer_load_dwordx4 v244, s[24:27], s49 offen offset:3072 lds
v_exp_f32_e64 v162, v162 clamp
ds_read_b64_tr_b16 v[108:109], v229 offset:0
v_exp_f32_e64 v163, v163 clamp
ds_read_b64_tr_b16 v[110:111], v229 offset:512
v_exp_f32_e64 v164, v164 clamp
ds_read_b64_tr_b16 v[112:113], v230 offset:0
s_add_u32 m0, s48, 8192
s_add_u32 s49, s49, 35840
s_cmp_eq_u32 s49, s65
s_cselect_b32 s49, s64, s49
buffer_load_dwordx4 v244, s[24:27], s49 offen offset:0 lds
v_exp_f32_e64 v165, v165 clamp
ds_read_b64_tr_b16 v[114:115], v230 offset:512
v_exp_f32_e64 v166, v166 clamp
ds_read_b64_tr_b16 v[116:117], v231 offset:0
v_exp_f32_e64 v167, v167 clamp
ds_read_b64_tr_b16 v[118:119], v231 offset:512
s_add_u32 s49, s49, 31744
buffer_load_dwordx4 v244, s[24:27], s49 offen offset:1024 lds
v_max_f32 v160, v160, v8
v_max_f32 v161, v161, v9
v_max_f32 v162, v162, v10
v_max_f32 v163, v163, v11
v_max_f32 v164, v164, v12
v_max_f32 v165, v165, v13
v_max_f32 v166, v166, v14
v_max_f32 v167, v167, v15
v_pk_add_f32 v[160:161], v[160:161], s[62:63] op_sel_hi:[1,0]
v_pk_add_f32 v[162:163], v[162:163], s[62:63] op_sel_hi:[1,0]
v_pk_add_f32 v[164:165], v[164:165], s[62:63] op_sel_hi:[1,0]
v_pk_add_f32 v[166:167], v[166:167], s[62:63] op_sel_hi:[1,0]
v_cvt_pk_bf16_f32 v8, v160, v161
v_cvt_pk_bf16_f32 v9, v162, v163
v_cvt_pk_bf16_f32 v10, v164, v165
v_cvt_pk_bf16_f32 v11, v166, v167
v_cvt_pk_bf16_f32 v60, v60, v61
v_cvt_pk_bf16_f32 v61, v62, v63
v_cvt_pk_bf16_f32 v62, v64, v65
v_cvt_pk_bf16_f32 v63, v66, v67
v_mfma_f32_16x16x32_bf16 v[188:191], v[8:11], v[144:147], v[188:191]
s_nop 0
v_mfma_f32_16x16x32_bf16 v[184:187], v[8:11], v[60:63], v[184:187]
v_pk_fma_f32 v[160:161], v[16:17], s[60:61], s[60:61] op_sel:[0,0,1] op_sel_hi:[1,0,1]
v_pk_fma_f32 v[162:163], v[18:19], s[60:61], s[60:61] op_sel:[0,0,1] op_sel_hi:[1,0,1]
v_pk_fma_f32 v[164:165], v[20:21], s[60:61], s[60:61] op_sel:[0,0,1] op_sel_hi:[1,0,1]
v_pk_fma_f32 v[166:167], v[22:23], s[60:61], s[60:61] op_sel:[0,0,1] op_sel_hi:[1,0,1]
v_exp_f32_e64 v160, v160 clamp
v_exp_f32_e64 v161, v161 clamp
v_exp_f32_e64 v162, v162 clamp
v_exp_f32_e64 v163, v163 clamp
v_exp_f32_e64 v164, v164 clamp
v_exp_f32_e64 v165, v165 clamp
v_exp_f32_e64 v166, v166 clamp
v_exp_f32_e64 v167, v167 clamp
v_max_f32 v160, v160, v16
v_max_f32 v161, v161, v17
v_max_f32 v162, v162, v18
v_max_f32 v163, v163, v19
v_max_f32 v164, v164, v20
v_max_f32 v165, v165, v21
v_max_f32 v166, v166, v22
v_max_f32 v167, v167, v23
v_pk_add_f32 v[160:161], v[160:161], s[62:63] op_sel_hi:[1,0]
v_pk_add_f32 v[162:163], v[162:163], s[62:63] op_sel_hi:[1,0]
v_pk_add_f32 v[164:165], v[164:165], s[62:63] op_sel_hi:[1,0]
v_pk_add_f32 v[166:167], v[166:167], s[62:63] op_sel_hi:[1,0]
v_cvt_pk_bf16_f32 v16, v160, v161
v_cvt_pk_bf16_f32 v17, v162, v163
v_cvt_pk_bf16_f32 v18, v164, v165
v_cvt_pk_bf16_f32 v19, v166, v167
v_cvt_pk_bf16_f32 v68, v68, v69
v_cvt_pk_bf16_f32 v69, v70, v71
v_cvt_pk_bf16_f32 v70, v72, v73
v_cvt_pk_bf16_f32 v71, v74, v75
v_mfma_f32_16x16x32_bf16 v[188:191], v[16:19], v[144:147], v[188:191]
s_nop 0
v_mfma_f32_16x16x32_bf16 v[184:187], v[16:19], v[68:71], v[184:187]
v_pk_fma_f32 v[160:161], v[24:25], s[60:61], s[60:61] op_sel:[0,0,1] op_sel_hi:[1,0,1]
v_pk_fma_f32 v[162:163], v[26:27], s[60:61], s[60:61] op_sel:[0,0,1] op_sel_hi:[1,0,1]
v_pk_fma_f32 v[164:165], v[28:29], s[60:61], s[60:61] op_sel:[0,0,1] op_sel_hi:[1,0,1]
v_pk_fma_f32 v[166:167], v[30:31], s[60:61], s[60:61] op_sel:[0,0,1] op_sel_hi:[1,0,1]
v_exp_f32_e64 v160, v160 clamp
v_exp_f32_e64 v161, v161 clamp
v_exp_f32_e64 v162, v162 clamp
v_exp_f32_e64 v163, v163 clamp
v_exp_f32_e64 v164, v164 clamp
v_exp_f32_e64 v165, v165 clamp
v_exp_f32_e64 v166, v166 clamp
v_exp_f32_e64 v167, v167 clamp
v_max_f32 v160, v160, v24
v_max_f32 v161, v161, v25
v_max_f32 v162, v162, v26
v_max_f32 v163, v163, v27
v_max_f32 v164, v164, v28
v_max_f32 v165, v165, v29
v_max_f32 v166, v166, v30
v_max_f32 v167, v167, v31
v_pk_add_f32 v[160:161], v[160:161], s[62:63] op_sel_hi:[1,0]
v_pk_add_f32 v[162:163], v[162:163], s[62:63] op_sel_hi:[1,0]
v_pk_add_f32 v[164:165], v[164:165], s[62:63] op_sel_hi:[1,0]
v_pk_add_f32 v[166:167], v[166:167], s[62:63] op_sel_hi:[1,0]
v_cvt_pk_bf16_f32 v24, v160, v161
v_cvt_pk_bf16_f32 v25, v162, v163
v_cvt_pk_bf16_f32 v26, v164, v165
v_cvt_pk_bf16_f32 v27, v166, v167
v_cvt_pk_bf16_f32 v76, v76, v77
v_cvt_pk_bf16_f32 v77, v78, v79
v_cvt_pk_bf16_f32 v78, v80, v81
v_cvt_pk_bf16_f32 v79, v82, v83
v_mfma_f32_16x16x32_bf16 v[188:191], v[24:27], v[144:147], v[188:191]
s_nop 0
v_mfma_f32_16x16x32_bf16 v[184:187], v[24:27], v[76:79], v[184:187]
v_pk_fma_f32 v[160:161], v[32:33], s[60:61], s[60:61] op_sel:[0,0,1] op_sel_hi:[1,0,1]
v_pk_fma_f32 v[162:163], v[34:35], s[60:61], s[60:61] op_sel:[0,0,1] op_sel_hi:[1,0,1]
v_pk_fma_f32 v[164:165], v[36:37], s[60:61], s[60:61] op_sel:[0,0,1] op_sel_hi:[1,0,1]
v_pk_fma_f32 v[166:167], v[38:39], s[60:61], s[60:61] op_sel:[0,0,1] op_sel_hi:[1,0,1]
v_exp_f32_e64 v160, v160 clamp
v_exp_f32_e64 v161, v161 clamp
v_exp_f32_e64 v162, v162 clamp
v_exp_f32_e64 v163, v163 clamp
v_exp_f32_e64 v164, v164 clamp
v_exp_f32_e64 v165, v165 clamp
v_exp_f32_e64 v166, v166 clamp
v_exp_f32_e64 v167, v167 clamp
v_max_f32 v160, v160, v32
v_max_f32 v161, v161, v33
v_max_f32 v162, v162, v34
v_max_f32 v163, v163, v35
v_max_f32 v164, v164, v36
v_max_f32 v165, v165, v37
v_max_f32 v166, v166, v38
v_max_f32 v167, v167, v39
v_pk_add_f32 v[160:161], v[160:161], s[62:63] op_sel_hi:[1,0]
v_pk_add_f32 v[162:163], v[162:163], s[62:63] op_sel_hi:[1,0]
v_pk_add_f32 v[164:165], v[164:165], s[62:63] op_sel_hi:[1,0]
v_pk_add_f32 v[166:167], v[166:167], s[62:63] op_sel_hi:[1,0]
v_cvt_pk_bf16_f32 v32, v160, v161
v_cvt_pk_bf16_f32 v33, v162, v163
v_cvt_pk_bf16_f32 v34, v164, v165
v_cvt_pk_bf16_f32 v35, v166, v167
v_cvt_pk_bf16_f32 v84, v84, v85
v_cvt_pk_bf16_f32 v85, v86, v87
v_cvt_pk_bf16_f32 v86, v88, v89
v_cvt_pk_bf16_f32 v87, v90, v91
v_mfma_f32_16x16x32_bf16 v[188:191], v[32:35], v[148:151], v[188:191]
s_nop 0
v_mfma_f32_16x16x32_bf16 v[184:187], v[32:35], v[84:87], v[184:187]
v_pk_fma_f32 v[160:161], v[40:41], s[60:61], s[60:61] op_sel:[0,0,1] op_sel_hi:[1,0,1]
v_pk_fma_f32 v[162:163], v[42:43], s[60:61], s[60:61] op_sel:[0,0,1] op_sel_hi:[1,0,1]
v_pk_fma_f32 v[164:165], v[44:45], s[60:61], s[60:61] op_sel:[0,0,1] op_sel_hi:[1,0,1]
v_pk_fma_f32 v[166:167], v[46:47], s[60:61], s[60:61] op_sel:[0,0,1] op_sel_hi:[1,0,1]
v_exp_f32_e64 v160, v160 clamp
v_exp_f32_e64 v161, v161 clamp
v_exp_f32_e64 v162, v162 clamp
v_exp_f32_e64 v163, v163 clamp
v_exp_f32_e64 v164, v164 clamp
v_exp_f32_e64 v165, v165 clamp
v_exp_f32_e64 v166, v166 clamp
v_exp_f32_e64 v167, v167 clamp
v_max_f32 v160, v160, v40
v_max_f32 v161, v161, v41
v_max_f32 v162, v162, v42
v_max_f32 v163, v163, v43
v_max_f32 v164, v164, v44
v_max_f32 v165, v165, v45
v_max_f32 v166, v166, v46
v_max_f32 v167, v167, v47
v_pk_add_f32 v[160:161], v[160:161], s[62:63] op_sel_hi:[1,0]
v_pk_add_f32 v[162:163], v[162:163], s[62:63] op_sel_hi:[1,0]
v_pk_add_f32 v[164:165], v[164:165], s[62:63] op_sel_hi:[1,0]
v_pk_add_f32 v[166:167], v[166:167], s[62:63] op_sel_hi:[1,0]
v_cvt_pk_bf16_f32 v40, v160, v161
v_cvt_pk_bf16_f32 v41, v162, v163
v_cvt_pk_bf16_f32 v42, v164, v165
v_cvt_pk_bf16_f32 v43, v166, v167
v_cvt_pk_bf16_f32 v92, v92, v93
v_cvt_pk_bf16_f32 v93, v94, v95
v_cvt_pk_bf16_f32 v94, v96, v97
v_cvt_pk_bf16_f32 v95, v98, v99
v_mfma_f32_16x16x32_bf16 v[188:191], v[40:43], v[152:155], v[188:191]
s_nop 0
v_mfma_f32_16x16x32_bf16 v[184:187], v[40:43], v[92:95], v[184:187]
v_pk_fma_f32 v[160:161], v[48:49], s[60:61], s[60:61] op_sel:[0,0,1] op_sel_hi:[1,0,1]
v_pk_fma_f32 v[162:163], v[50:51], s[60:61], s[60:61] op_sel:[0,0,1] op_sel_hi:[1,0,1]
v_exp_f32_e64 v160, v160 clamp
v_exp_f32_e64 v161, v161 clamp
v_exp_f32_e64 v162, v162 clamp
v_exp_f32_e64 v163, v163 clamp
v_max_f32 v160, v160, v48
v_max_f32 v161, v161, v49
v_max_f32 v162, v162, v50
v_max_f32 v163, v163, v51
v_pk_add_f32 v[160:161], v[160:161], s[62:63] op_sel_hi:[1,0]
v_pk_add_f32 v[162:163], v[162:163], s[62:63] op_sel_hi:[1,0]
v_cvt_pk_bf16_f32 v48, v160, v161
v_cvt_pk_bf16_f32 v49, v162, v163
v_pk_mov_b32 v[50:51], 0, 0
v_cvt_pk_bf16_f32 v100, v100, v101
v_cvt_pk_bf16_f32 v101, v102, v103
v_pk_mov_b32 v[102:103], 0, 0
v_mfma_f32_16x16x32_bf16 v[188:191], v[48:51], v[156:159], v[188:191]
s_nop 0
v_mfma_f32_16x16x32_bf16 v[184:187], v[48:51], v[100:103], v[184:187]
s_nop 5
v_cvt_pk_bf16_f32 v172, v188, v189
v_cvt_pk_bf16_f32 v174, v190, v191
v_cvt_pk_bf16_f32 v168, v184, v185
v_cvt_pk_bf16_f32 v170, v186, v187
buffer_store_dwordx4 v[168:171], v241, s[12:15], s53 offen sc1
s_mov_b32 exec_lo, 0x10001
s_mov_b32 exec_hi, 0x10001
buffer_store_dwordx4 v[172:175], v176, s[12:15], s53 offen offset:1024 sc1
s_mov_b64 exec, -1
s_add_u32 s49, s49, 31744
buffer_load_dwordx4 v244, s[24:27], s49 offen offset:2048 lds
s_add_u32 s49, s49, 31744
buffer_load_dwordx4 v244, s[24:27], s49 offen offset:3072 lds
s_add_u32 m0, s48, 12288
s_add_u32 s49, s49, 35840
s_cmp_eq_u32 s49, s65
s_cselect_b32 s49, s64, s49
buffer_load_dwordx4 v244, s[24:27], s49 offen offset:0 lds
s_add_u32 s49, s49, 31744
buffer_load_dwordx4 v244, s[24:27], s49 offen offset:1024 lds
s_waitcnt lgkmcnt(6)
v_mfma_f32_16x16x32_bf16 v[144:147], v[104:107], a[0:3], 1.0
ds_read_b64_tr_b16 v[120:121], v228 offset:4096
v_mfma_f32_16x16x32_bf16 v[160:163], v[104:107], a[4:7], 1.0
ds_read_b64_tr_b16 v[122:123], v228 offset:4608
v_mfma_f32_16x16x32_bf16 v[176:179], v[104:107], a[8:11], 1.0
ds_read_b64_tr_b16 v[124:125], v229 offset:4096
v_mfma_f32_16x16x32_bf16 v[192:195], v[104:107], a[12:15], 1.0
ds_read_b64_tr_b16 v[126:127], v229 offset:4608
s_waitcnt lgkmcnt(8)
v_mfma_f32_16x16x32_bf16 v[148:151], v[108:111], a[0:3], 1.0
ds_read_b64_tr_b16 v[128:129], v230 offset:4096
v_mfma_f32_16x16x32_bf16 v[164:167], v[108:111], a[4:7], 1.0
ds_read_b64_tr_b16 v[130:131], v230 offset:4608
v_mfma_f32_16x16x32_bf16 v[180:183], v[108:111], a[8:11], 1.0
ds_read_b64_tr_b16 v[132:133], v231 offset:4096
s_waitcnt lgkmcnt(9)
v_mfma_f32_16x16x32_bf16 v[152:155], v[112:115], a[0:3], 1.0
ds_read_b64_tr_b16 v[134:135], v231 offset:4608
v_mfma_f32_16x16x32_bf16 v[168:171], v[112:115], a[4:7], 1.0
s_add_u32 s49, s49, 31744
buffer_load_dwordx4 v244, s[24:27], s49 offen offset:2048 lds
v_mfma_f32_16x16x32_bf16 v[184:187], v[112:115], a[8:11], 1.0
s_waitcnt lgkmcnt(8)
v_mfma_f32_16x16x32_bf16 v[156:159], v[116:119], a[0:3], 1.0
s_add_u32 s49, s49, 31744
buffer_load_dwordx4 v244, s[24:27], s49 offen offset:3072 lds
v_mfma_f32_16x16x32_bf16 v[172:175], v[116:119], a[4:7], 1.0
v_mfma_f32_16x16x32_bf16 v[188:191], v[116:119], a[8:11], 1.0
s_add_u32 m0, s48, 16384
s_add_u32 s49, s49, 35840
s_cmp_eq_u32 s49, s65
s_cselect_b32 s49, s64, s49
buffer_load_dwordx4 v244, s[24:27], s49 offen offset:0 lds
s_waitcnt lgkmcnt(6)
v_mfma_f32_16x16x32_bf16 v[144:147], v[120:123], a[16:19], v[144:147]
ds_read_b64_tr_b16 v[104:105], v228 offset:8192
s_and_b32 s51, s69, 1
v_mfma_f32_16x16x32_bf16 v[160:163], v[120:123], a[20:23], v[160:163]
ds_read_b64_tr_b16 v[106:107], v228 offset:8704
s_lshl_b32 s51, s51, 5
v_mfma_f32_16x16x32_bf16 v[176:179], v[120:123], a[24:27], v[176:179]
ds_read_b64_tr_b16 v[108:109], v229 offset:8192
s_lshl_b32 s52, s57, 6
v_mfma_f32_16x16x32_bf16 v[192:195], v[120:123], a[28:31], v[192:195]
ds_read_b64_tr_b16 v[110:111], v229 offset:8704
s_add_u32 s51, s51, s52
s_waitcnt lgkmcnt(8)
v_mfma_f32_16x16x32_bf16 v[148:151], v[124:127], a[16:19], v[148:151]
ds_read_b64_tr_b16 v[112:113], v230 offset:8192
v_and_b32 v254, 31, v240
v_mfma_f32_16x16x32_bf16 v[164:167], v[124:127], a[20:23], v[164:167]
ds_read_b64_tr_b16 v[114:115], v230 offset:8704
v_add_u32 v254, s51, v254
v_mfma_f32_16x16x32_bf16 v[180:183], v[124:127], a[24:27], v[180:183]
ds_read_b64_tr_b16 v[116:117], v231 offset:8192
v_lshlrev_b32 v255, 1, v254
s_waitcnt lgkmcnt(9)
v_mfma_f32_16x16x32_bf16 v[152:155], v[128:131], a[16:19], v[152:155]
ds_read_b64_tr_b16 v[118:119], v231 offset:8704
buffer_load_ushort v220, v255, s[36:39], 0 offen
v_mfma_f32_16x16x32_bf16 v[168:171], v[128:131], a[20:23], v[168:171]
s_bfe_u32 s54, s69, 0x10000
s_lshl_b32 s54, s54, 6
v_lshrrev_b32 v248, 5, v240
v_lshl_add_u32 v248, v248, 9, s48
v_xad_u32 v247, v242, s54, v248
v_xor_b32 v248, 64, v247
s_waitcnt vmcnt(18)
ds_read_b64_tr_b16 a[0:1], v247 offset:0
s_add_u32 s49, s49, 31744
buffer_load_dwordx4 v244, s[24:27], s49 offen offset:1024 lds
v_mfma_f32_16x16x32_bf16 v[184:187], v[128:131], a[24:27], v[184:187]
ds_read_b64_tr_b16 a[2:3], v247 offset:1024
ds_read_b64_tr_b16 a[4:5], v248 offset:0
s_waitcnt lgkmcnt(11)
v_mfma_f32_16x16x32_bf16 v[156:159], v[132:135], a[16:19], v[156:159]
ds_read_b64_tr_b16 a[6:7], v248 offset:1024
s_add_u32 s49, s49, 31744
buffer_load_dwordx4 v244, s[24:27], s49 offen offset:2048 lds
v_mfma_f32_16x16x32_bf16 v[172:175], v[132:135], a[20:23], v[172:175]
s_waitcnt vmcnt(18)
ds_read_b64_tr_b16 a[8:9], v247 offset:2048
ds_read_b64_tr_b16 a[10:11], v247 offset:3072
v_mfma_f32_16x16x32_bf16 v[188:191], v[132:135], a[24:27], v[188:191]
ds_read_b64_tr_b16 a[12:13], v248 offset:2048
s_add_u32 s49, s49, 31744
buffer_load_dwordx4 v244, s[24:27], s49 offen offset:3072 lds
s_waitcnt lgkmcnt(13)
v_mfma_f32_16x16x32_bf16 v[144:147], v[104:107], a[32:35], v[144:147]
ds_read_b64_tr_b16 v[120:121], v228 offset:12288
v_lshrrev_b32 v218, 3, v254
v_mfma_f32_16x16x32_bf16 v[160:163], v[104:107], a[36:39], v[160:163]
ds_read_b64_tr_b16 v[122:123], v228 offset:12800
v_and_b32 v219, 7, v254
v_mfma_f32_16x16x32_bf16 v[176:179], v[104:107], a[40:43], v[176:179]
ds_read_b64_tr_b16 v[124:125], v229 offset:12288
v_lshlrev_b32 v219, 2, v219
v_mfma_f32_16x16x32_bf16 v[192:195], v[104:107], a[44:47], v[192:195]
ds_read_b64_tr_b16 v[126:127], v229 offset:12800
v_lshl_add_u32 v255, v218, 7, v219
s_waitcnt lgkmcnt(15)
v_mfma_f32_16x16x32_bf16 v[148:151], v[108:111], a[32:35], v[148:151]
ds_read_b64_tr_b16 v[128:129], v230 offset:12288
s_mul_i32 s51, s69, 22
v_mfma_f32_16x16x32_bf16 v[164:167], v[108:111], a[36:39], v[164:167]
ds_read_b64_tr_b16 v[130:131], v230 offset:12800
s_lshr_b32 s52, s51, 2
v_mfma_f32_16x16x32_bf16 v[180:183], v[108:111], a[40:43], v[180:183]
ds_read_b64_tr_b16 v[132:133], v231 offset:12288
s_mul_i32 s67, s58, 22
s_waitcnt lgkmcnt(15)
v_mfma_f32_16x16x32_bf16 v[152:155], v[112:115], a[32:35], v[152:155]
ds_read_b64_tr_b16 v[134:135], v231 offset:12800
s_add_u32 s52, s52, s67
v_mfma_f32_16x16x32_bf16 v[168:171], v[112:115], a[36:39], v[168:171]
ds_read_b64_tr_b16 a[14:15], v248 offset:3072
s_add_u32 m0, s48, 20480
s_add_u32 s49, s49, 35840
s_cmp_eq_u32 s49, s65
s_cselect_b32 s49, s64, s49
buffer_load_dwordx4 v244, s[24:27], s49 offen offset:0 lds
v_mfma_f32_16x16x32_bf16 v[184:187], v[112:115], a[40:43], v[184:187]
s_waitcnt vmcnt(18)
ds_read_b64_tr_b16 a[16:17], v247 offset:4096
ds_read_b64_tr_b16 a[18:19], v247 offset:5120
s_waitcnt lgkmcnt(15)
v_mfma_f32_16x16x32_bf16 v[156:159], v[116:119], a[32:35], v[156:159]
ds_read_b64_tr_b16 a[20:21], v248 offset:4096
s_add_u32 s49, s49, 31744
buffer_load_dwordx4 v244, s[24:27], s49 offen offset:1024 lds
v_mfma_f32_16x16x32_bf16 v[172:175], v[116:119], a[36:39], v[172:175]
ds_read_b64_tr_b16 a[22:23], v248 offset:5120
s_waitcnt vmcnt(17)
ds_read_b64_tr_b16 a[24:25], v247 offset:6144
v_mfma_f32_16x16x32_bf16 v[188:191], v[116:119], a[40:43], v[188:191]
ds_read_b64_tr_b16 a[26:27], v247 offset:7168
s_add_u32 s49, s49, 31744
buffer_load_dwordx4 v244, s[24:27], s49 offen offset:2048 lds
s_waitcnt lgkmcnt(13)
v_mfma_f32_16x16x32_bf16 v[144:147], v[120:123], a[48:51], v[144:147]
ds_read_b64_tr_b16 v[104:105], v228 offset:16384
s_add_u32 s52, s52, 288
v_mfma_f32_16x16x32_bf16 v[160:163], v[120:123], a[52:55], v[160:163]
ds_read_b64_tr_b16 v[106:107], v228 offset:16896
v_min_u32 v218, 5, v240
v_mfma_f32_16x16x32_bf16 v[176:179], v[120:123], a[56:59], v[176:179]
ds_read_b64_tr_b16 v[108:109], v229 offset:16384
v_add_lshl_u32 v222, v218, s52, 7
v_mfma_f32_16x16x32_bf16 v[192:195], v[120:123], a[60:63], v[192:195]
ds_read_b64_tr_b16 v[110:111], v229 offset:16896
s_mul_i32 s67, s58, 88
s_waitcnt lgkmcnt(15)
v_mfma_f32_16x16x32_bf16 v[148:151], v[124:127], a[48:51], v[148:151]
ds_read_b64_tr_b16 v[112:113], v230 offset:16384
s_add_u32 s51, s51, s67
v_mfma_f32_16x16x32_bf16 v[164:167], v[124:127], a[52:55], v[164:167]
ds_read_b64_tr_b16 v[114:115], v230 offset:16896
s_lshl_b32 s51, s51, 11
v_mfma_f32_16x16x32_bf16 v[180:183], v[124:127], a[56:59], v[180:183]
ds_read_b64_tr_b16 v[116:117], v231 offset:16384
s_add_u32 s59, s51, 3145728
s_waitcnt lgkmcnt(15)
v_mfma_f32_16x16x32_bf16 v[152:155], v[128:131], a[48:51], v[152:155]
ds_read_b64_tr_b16 v[118:119], v231 offset:16896
s_mov_b32 s66, s59
v_mfma_f32_16x16x32_bf16 v[168:171], v[128:131], a[52:55], v[168:171]
ds_read_b64_tr_b16 a[28:29], v248 offset:6144
s_add_u32 s49, s49, 31744
buffer_load_dwordx4 v244, s[24:27], s49 offen offset:3072 lds
v_mfma_f32_16x16x32_bf16 v[184:187], v[128:131], a[56:59], v[184:187]
ds_read_b64_tr_b16 a[30:31], v248 offset:7168
s_waitcnt vmcnt(17)
ds_read_b64_tr_b16 a[32:33], v247 offset:8192
s_waitcnt lgkmcnt(15)
v_mfma_f32_16x16x32_bf16 v[156:159], v[132:135], a[48:51], v[156:159]
ds_read_b64_tr_b16 a[34:35], v247 offset:9216
s_add_u32 m0, s48, 0
s_add_u32 s49, s49, 35840
s_cmp_eq_u32 s49, s65
s_cselect_b32 s49, s64, s49
buffer_load_dwordx4 v244, s[24:27], s49 offen offset:0 lds
v_mfma_f32_16x16x32_bf16 v[172:175], v[132:135], a[52:55], v[172:175]
ds_read_b64_tr_b16 a[36:37], v248 offset:8192
ds_read_b64_tr_b16 a[38:39], v248 offset:9216
v_mfma_f32_16x16x32_bf16 v[188:191], v[132:135], a[56:59], v[188:191]
s_waitcnt vmcnt(14)
ds_read_b64_tr_b16 a[40:41], v247 offset:10240
s_add_u32 s49, s49, 31744
buffer_load_dwordx4 v244, s[24:27], s49 offen offset:1024 lds
s_waitcnt lgkmcnt(13)
v_mfma_f32_16x16x32_bf16 v[144:147], v[104:107], a[64:67], v[144:147]
ds_read_b64_tr_b16 v[120:121], v228 offset:20480
s_add_u32 s68, s59, 45056
v_mfma_f32_16x16x32_bf16 v[160:163], v[104:107], a[68:71], v[160:163]
ds_read_b64_tr_b16 v[122:123], v228 offset:20992
s_bfe_u32 s51, s45, 0x40004
v_mfma_f32_16x16x32_bf16 v[176:179], v[104:107], a[72:75], v[176:179]
ds_read_b64_tr_b16 v[124:125], v229 offset:20480
s_mul_i32 s51, s51, 4096
v_mfma_f32_16x16x32_bf16 v[192:195], v[104:107], a[76:79], v[192:195]
ds_read_b64_tr_b16 v[126:127], v229 offset:20992
s_add_u32 s59, s59, s51
s_waitcnt lgkmcnt(15)
v_mfma_f32_16x16x32_bf16 v[148:151], v[108:111], a[64:67], v[148:151]
ds_read_b64_tr_b16 v[128:129], v230 offset:20480
s_bfe_u32 s51, s69, 0x10001
v_mfma_f32_16x16x32_bf16 v[164:167], v[108:111], a[68:71], v[164:167]
ds_read_b64_tr_b16 v[130:131], v230 offset:20992
s_lshl_b32 s51, s51, 10
v_mfma_f32_16x16x32_bf16 v[180:183], v[108:111], a[72:75], v[180:183]
ds_read_b64_tr_b16 v[132:133], v231 offset:20480
v_add_u32 v245, s51, v241
s_waitcnt lgkmcnt(15)
v_mfma_f32_16x16x32_bf16 v[152:155], v[112:115], a[64:67], v[152:155]
ds_read_b64_tr_b16 v[134:135], v231 offset:20992
v_xor_b32 v246, 0x400, v245
v_mfma_f32_16x16x32_bf16 v[168:171], v[112:115], a[68:71], v[168:171]
ds_read_b64_tr_b16 a[42:43], v247 offset:11264
s_add_u32 s49, s49, 31744
buffer_load_dwordx4 v244, s[24:27], s49 offen offset:2048 lds
v_mfma_f32_16x16x32_bf16 v[184:187], v[112:115], a[72:75], v[184:187]
ds_read_b64_tr_b16 a[44:45], v248 offset:10240
ds_read_b64_tr_b16 a[46:47], v248 offset:11264
s_waitcnt lgkmcnt(15)
v_mfma_f32_16x16x32_bf16 v[156:159], v[116:119], a[64:67], v[156:159]
s_waitcnt vmcnt(14)
ds_read_b64_tr_b16 a[48:49], v247 offset:12288
s_add_u32 s49, s49, 31744
buffer_load_dwordx4 v244, s[24:27], s49 offen offset:3072 lds
v_mfma_f32_16x16x32_bf16 v[172:175], v[116:119], a[68:71], v[172:175]
ds_read_b64_tr_b16 a[50:51], v247 offset:13312
ds_read_b64_tr_b16 a[52:53], v248 offset:12288
v_mfma_f32_16x16x32_bf16 v[188:191], v[116:119], a[72:75], v[188:191]
ds_read_b64_tr_b16 a[54:55], v248 offset:13312
s_add_u32 m0, s48, 4096
s_add_u32 s49, s49, 35840
s_cmp_eq_u32 s49, s65
s_cselect_b32 s49, s64, s49
buffer_load_dwordx4 v244, s[24:27], s49 offen offset:0 lds
s_barrier
s_waitcnt lgkmcnt(13)
v_mfma_f32_16x16x32_bf16 v[144:147], v[120:123], a[80:83], v[144:147]
ds_read_b64_tr_b16 v[104:105], v228 offset:24576
v_xor_b32 v221, 0x200, v255
v_mfma_f32_16x16x32_bf16 v[160:163], v[120:123], a[84:87], v[160:163]
ds_read_b64_tr_b16 v[106:107], v228 offset:25088
s_bfe_u32 s51, s44, 0x40011
v_mfma_f32_16x16x32_bf16 v[176:179], v[120:123], a[88:91], v[176:179]
ds_read_b64_tr_b16 v[108:109], v229 offset:24576
s_bfe_u32 s52, s44, 0x40015
v_mfma_f32_16x16x32_bf16 v[192:195], v[120:123], a[92:95], v[192:195]
ds_read_b64_tr_b16 v[110:111], v229 offset:25088
s_sub_u32 s52, s52, s51
s_waitcnt lgkmcnt(15)
v_mfma_f32_16x16x32_bf16 v[148:151], v[124:127], a[80:83], v[148:151]
ds_read_b64_tr_b16 v[112:113], v230 offset:24576
s_lshl_b32 s52, s52, 2
v_mfma_f32_16x16x32_bf16 v[164:167], v[124:127], a[84:87], v[164:167]
ds_read_b64_tr_b16 v[114:115], v230 offset:25088
s_lshl_b32 s51, s51, 3
v_mfma_f32_16x16x32_bf16 v[180:183], v[124:127], a[88:91], v[180:183]
ds_read_b64_tr_b16 v[116:117], v231 offset:24576
s_lshl_b32 s67, s69, 1
s_waitcnt lgkmcnt(15)
v_mfma_f32_16x16x32_bf16 v[152:155], v[128:131], a[80:83], v[152:155]
ds_read_b64_tr_b16 v[118:119], v231 offset:25088
s_add_u32 s51, s51, s67
v_mfma_f32_16x16x32_bf16 v[168:171], v[128:131], a[84:87], v[168:171]
s_waitcnt vmcnt(14)
ds_read_b64_tr_b16 a[56:57], v247 offset:14336
s_add_u32 s49, s49, 31744
buffer_load_dwordx4 v244, s[24:27], s49 offen offset:1024 lds
v_mfma_f32_16x16x32_bf16 v[184:187], v[128:131], a[88:91], v[184:187]
ds_read_b64_tr_b16 a[58:59], v247 offset:15360
ds_read_b64_tr_b16 a[60:61], v248 offset:14336
s_waitcnt lgkmcnt(15)
v_mfma_f32_16x16x32_bf16 v[156:159], v[132:135], a[80:83], v[156:159]
ds_read_b64_tr_b16 a[62:63], v248 offset:15360
s_add_u32 s49, s49, 31744
buffer_load_dwordx4 v244, s[24:27], s49 offen offset:2048 lds
v_mfma_f32_16x16x32_bf16 v[172:175], v[132:135], a[84:87], v[172:175]
s_waitcnt vmcnt(13)
ds_read_b64_tr_b16 a[64:65], v247 offset:16384
ds_read_b64_tr_b16 a[66:67], v247 offset:17408
v_mfma_f32_16x16x32_bf16 v[188:191], v[132:135], a[88:91], v[188:191]
ds_read_b64_tr_b16 a[68:69], v248 offset:16384
s_add_u32 s49, s49, 31744
buffer_load_dwordx4 v244, s[24:27], s49 offen offset:3072 lds
s_waitcnt lgkmcnt(13)
v_mfma_f32_16x16x32_bf16 v[144:147], v[104:107], a[96:99], v[144:147]
ds_read_b64_tr_b16 v[120:121], v228 offset:28672
s_add_u32 s51, s51, 480
v_mfma_f32_16x16x32_bf16 v[160:163], v[104:107], a[100:103], v[160:163]
ds_read_b64_tr_b16 v[122:123], v228 offset:29184
v_and_b32 v249, 2, v240
v_mfma_f32_16x16x32_bf16 v[176:179], v[104:107], a[104:107], v[176:179]
ds_read_b64_tr_b16 v[124:125], v229 offset:28672
v_and_b32 v218, 1, v240
v_mfma_f32_16x16x32_bf16 v[192:195], v[104:107], a[108:111], v[192:195]
ds_read_b64_tr_b16 v[126:127], v229 offset:29184
v_mad_u32_u24 v249, v249, s52, v218
s_waitcnt lgkmcnt(15)
v_mfma_f32_16x16x32_bf16 v[148:151], v[108:111], a[96:99], v[148:151]
ds_read_b64_tr_b16 v[128:129], v230 offset:28672
v_add_lshl_u32 v249, v249, s51, 7
v_mfma_f32_16x16x32_bf16 v[164:167], v[108:111], a[100:103], v[164:167]
ds_read_b64_tr_b16 v[130:131], v230 offset:29184
v_and_b32 v216, 15, v240
v_mfma_f32_16x16x32_bf16 v[180:183], v[108:111], a[104:107], v[180:183]
ds_read_b64_tr_b16 v[132:133], v231 offset:28672
v_cmp_gt_u32 vcc, 10, v216
s_waitcnt lgkmcnt(15)
v_mfma_f32_16x16x32_bf16 v[152:155], v[112:115], a[96:99], v[152:155]
ds_read_b64_tr_b16 v[134:135], v231 offset:29184
v_cndmask_b32_e64 v216, 0, -1, vcc
v_mfma_f32_16x16x32_bf16 v[168:171], v[112:115], a[100:103], v[168:171]
ds_read_b64_tr_b16 a[70:71], v248 offset:17408
s_add_u32 m0, s48, 8192
s_add_u32 s49, s49, 35840
s_cmp_eq_u32 s49, s65
s_cselect_b32 s49, s64, s49
buffer_load_dwordx4 v244, s[24:27], s49 offen offset:0 lds
v_mfma_f32_16x16x32_bf16 v[184:187], v[112:115], a[104:107], v[184:187]
s_waitcnt vmcnt(13)
ds_read_b64_tr_b16 a[72:73], v247 offset:18432
ds_read_b64_tr_b16 a[74:75], v247 offset:19456
s_waitcnt lgkmcnt(15)
v_mfma_f32_16x16x32_bf16 v[156:159], v[116:119], a[96:99], v[156:159]
ds_read_b64_tr_b16 a[76:77], v248 offset:18432
s_add_u32 s49, s49, 31744
buffer_load_dwordx4 v244, s[24:27], s49 offen offset:1024 lds
v_mfma_f32_16x16x32_bf16 v[172:175], v[116:119], a[100:103], v[172:175]
ds_read_b64_tr_b16 a[78:79], v248 offset:19456
s_waitcnt vmcnt(12)
ds_read_b64_tr_b16 a[80:81], v247 offset:20480
v_mfma_f32_16x16x32_bf16 v[188:191], v[116:119], a[104:107], v[188:191]
ds_read_b64_tr_b16 a[82:83], v247 offset:21504
s_add_u32 s49, s49, 31744
buffer_load_dwordx4 v244, s[24:27], s49 offen offset:2048 lds
s_waitcnt lgkmcnt(13)
v_mfma_f32_16x16x32_bf16 v[144:147], v[120:123], a[112:115], v[144:147]
ds_read_b64_tr_b16 v[104:105], v228 offset:32768
s_lshl_b32 s51, s69, 10
s_add_u32 s51, s51, 98304
v_add_u32 v224, s51, v241
v_mfma_f32_16x16x32_bf16 v[160:163], v[120:123], a[116:119], v[160:163]
ds_read_b64_tr_b16 v[106:107], v228 offset:33280
v_xor_b32 v225, 1024, v224
v_mfma_f32_16x16x32_bf16 v[176:179], v[120:123], a[120:123], v[176:179]
ds_read_b64_tr_b16 v[108:109], v229 offset:32768
v_xor_b32 v226, 2048, v224
v_mfma_f32_16x16x32_bf16 v[192:195], v[120:123], a[124:127], v[192:195]
ds_read_b64_tr_b16 v[110:111], v229 offset:33280
v_xor_b32 v227, 3072, v224
s_waitcnt lgkmcnt(15)
v_mfma_f32_16x16x32_bf16 v[148:151], v[124:127], a[112:115], v[148:151]
ds_read_b64_tr_b16 v[112:113], v230 offset:32768
s_mov_b32 s52, 0x3f80
s_mov_b32 s67, 0x3f800000
v_mfma_f32_16x16x32_bf16 v[164:167], v[124:127], a[116:119], v[164:167]
ds_read_b64_tr_b16 v[114:115], v230 offset:33280
v_mfma_f32_16x16x32_bf16 v[180:183], v[124:127], a[120:123], v[180:183]
ds_read_b64_tr_b16 v[116:117], v231 offset:32768
s_waitcnt lgkmcnt(15)
v_mfma_f32_16x16x32_bf16 v[152:155], v[128:131], a[112:115], v[152:155]
ds_read_b64_tr_b16 v[118:119], v231 offset:33280
v_mfma_f32_16x16x32_bf16 v[168:171], v[128:131], a[116:119], v[168:171]
ds_read_b64_tr_b16 a[84:85], v248 offset:20480
s_add_u32 s49, s49, 31744
buffer_load_dwordx4 v244, s[24:27], s49 offen offset:3072 lds
v_mfma_f32_16x16x32_bf16 v[184:187], v[128:131], a[120:123], v[184:187]
ds_read_b64_tr_b16 a[86:87], v248 offset:21504
s_waitcnt vmcnt(12)
ds_read_b64_tr_b16 a[88:89], v247 offset:22528
s_waitcnt lgkmcnt(15)
v_mfma_f32_16x16x32_bf16 v[156:159], v[132:135], a[112:115], v[156:159]
ds_read_b64_tr_b16 a[90:91], v247 offset:23552
s_add_u32 m0, s48, 12288
s_add_u32 s49, s49, 35840
s_cmp_eq_u32 s49, s65
s_cselect_b32 s49, s64, s49
buffer_load_dwordx4 v244, s[24:27], s49 offen offset:0 lds
v_mfma_f32_16x16x32_bf16 v[172:175], v[132:135], a[116:119], v[172:175]
ds_read_b64_tr_b16 a[92:93], v248 offset:22528
ds_read_b64_tr_b16 a[94:95], v248 offset:23552
v_mfma_f32_16x16x32_bf16 v[188:191], v[132:135], a[120:123], v[188:191]
s_waitcnt vmcnt(11)
ds_read_b64_tr_b16 a[96:97], v247 offset:0
s_add_u32 s49, s49, 31744
buffer_load_dwordx4 v244, s[24:27], s49 offen offset:1024 lds
s_waitcnt lgkmcnt(13)
v_mfma_f32_16x16x32_bf16 v[144:147], v[104:107], a[128:131], v[144:147]
ds_read_b64_tr_b16 v[120:121], v228 offset:36864
v_mfma_f32_16x16x32_bf16 v[160:163], v[104:107], a[132:135], v[160:163]
ds_read_b64_tr_b16 v[122:123], v228 offset:37376
v_mfma_f32_16x16x32_bf16 v[176:179], v[104:107], a[136:139], v[176:179]
ds_read_b64_tr_b16 v[124:125], v229 offset:36864
v_mfma_f32_16x16x32_bf16 v[192:195], v[104:107], a[140:143], v[192:195]
ds_read_b64_tr_b16 v[126:127], v229 offset:37376
s_waitcnt lgkmcnt(15)
v_mfma_f32_16x16x32_bf16 v[148:151], v[108:111], a[128:131], v[148:151]
ds_read_b64_tr_b16 v[128:129], v230 offset:36864
v_mfma_f32_16x16x32_bf16 v[164:167], v[108:111], a[132:135], v[164:167]
ds_read_b64_tr_b16 v[130:131], v230 offset:37376
v_mfma_f32_16x16x32_bf16 v[180:183], v[108:111], a[136:139], v[180:183]
ds_read_b64_tr_b16 v[132:133], v231 offset:36864
s_waitcnt lgkmcnt(15)
v_mfma_f32_16x16x32_bf16 v[152:155], v[112:115], a[128:131], v[152:155]
ds_read_b64_tr_b16 v[134:135], v231 offset:37376
v_mfma_f32_16x16x32_bf16 v[168:171], v[112:115], a[132:135], v[168:171]
ds_read_b64_tr_b16 a[98:99], v247 offset:1024
v_mfma_f32_16x16x32_bf16 v[184:187], v[112:115], a[136:139], v[184:187]
ds_read_b64_tr_b16 a[100:101], v248 offset:0
ds_read_b64_tr_b16 a[102:103], v248 offset:1024
s_waitcnt lgkmcnt(15)
v_mfma_f32_16x16x32_bf16 v[156:159], v[116:119], a[128:131], v[156:159]
s_waitcnt vmcnt(10)
ds_read_b64_tr_b16 a[104:105], v247 offset:2048
v_mfma_f32_16x16x32_bf16 v[172:175], v[116:119], a[132:135], v[172:175]
ds_read_b64_tr_b16 a[106:107], v247 offset:3072
ds_read_b64_tr_b16 a[108:109], v248 offset:2048
v_mfma_f32_16x16x32_bf16 v[188:191], v[116:119], a[136:139], v[188:191]
ds_read_b64_tr_b16 a[110:111], v248 offset:3072
s_waitcnt lgkmcnt(13)
v_mfma_f32_16x16x32_bf16 v[144:147], v[120:123], a[144:147], v[144:147]
ds_read_b64_tr_b16 v[104:105], v228 offset:40960
v_mfma_f32_16x16x32_bf16 v[160:163], v[120:123], a[148:151], v[160:163]
s_lshl_b32 s51, s63, 11
s_add_u32 s51, s51, 262144
buffer_load_dwordx4 v[40:43], v239, s[12:15], s51 offen offset:1024 sc1
ds_read_b64_tr_b16 v[106:107], v228 offset:41472
v_mfma_f32_16x16x32_bf16 v[176:179], v[120:123], a[152:155], v[176:179]
ds_read_b64_tr_b16 v[108:109], v229 offset:40960
v_mfma_f32_16x16x32_bf16 v[192:195], v[120:123], a[156:159], v[192:195]
buffer_load_dwordx4 v[0:3], v241, s[12:15], s51 offen offset:0 sc1
ds_read_b64_tr_b16 v[110:111], v229 offset:41472
s_waitcnt lgkmcnt(15)
v_mfma_f32_16x16x32_bf16 v[148:151], v[124:127], a[144:147], v[148:151]
ds_read_b64_tr_b16 v[112:113], v230 offset:40960
v_mfma_f32_16x16x32_bf16 v[164:167], v[124:127], a[148:151], v[164:167]
buffer_load_dwordx4 v[4:7], v241, s[12:15], s51 offen offset:2048 sc1
ds_read_b64_tr_b16 v[114:115], v230 offset:41472
v_mfma_f32_16x16x32_bf16 v[180:183], v[124:127], a[152:155], v[180:183]
ds_read_b64_tr_b16 v[116:117], v231 offset:40960
s_waitcnt lgkmcnt(15)
v_mfma_f32_16x16x32_bf16 v[152:155], v[128:131], a[144:147], v[152:155]
s_add_u32 s51, s51, 4096
buffer_load_dwordx4 v[8:11], v241, s[12:15], s51 offen offset:0 sc1
ds_read_b64_tr_b16 v[118:119], v231 offset:41472
v_mfma_f32_16x16x32_bf16 v[168:171], v[128:131], a[148:151], v[168:171]
s_waitcnt vmcnt(12)
ds_read_b64_tr_b16 a[112:113], v247 offset:4096
v_mfma_f32_16x16x32_bf16 v[184:187], v[128:131], a[152:155], v[184:187]
buffer_load_dwordx4 v[12:15], v241, s[12:15], s51 offen offset:2048 sc1
ds_read_b64_tr_b16 a[114:115], v247 offset:5120
ds_read_b64_tr_b16 a[116:117], v248 offset:4096
s_waitcnt lgkmcnt(15)
v_mfma_f32_16x16x32_bf16 v[156:159], v[132:135], a[144:147], v[156:159]
ds_read_b64_tr_b16 a[118:119], v248 offset:5120
v_mfma_f32_16x16x32_bf16 v[172:175], v[132:135], a[148:151], v[172:175]
s_add_u32 s51, s51, 4096
buffer_load_dwordx4 v[16:19], v241, s[12:15], s51 offen offset:0 sc1
s_waitcnt vmcnt(12)
ds_read_b64_tr_b16 a[120:121], v247 offset:6144
ds_read_b64_tr_b16 a[122:123], v247 offset:7168
v_mfma_f32_16x16x32_bf16 v[188:191], v[132:135], a[152:155], v[188:191]
ds_read_b64_tr_b16 a[124:125], v248 offset:6144
s_waitcnt lgkmcnt(13)
v_mfma_f32_16x16x32_bf16 v[144:147], v[104:107], a[160:163], v[144:147]
buffer_load_dwordx4 v[20:23], v241, s[12:15], s51 offen offset:2048 sc1
ds_read_b64_tr_b16 v[120:121], v228 offset:45056
v_mfma_f32_16x16x32_bf16 v[160:163], v[104:107], a[164:167], v[160:163]
ds_read_b64_tr_b16 v[122:123], v228 offset:45568
v_mfma_f32_16x16x32_bf16 v[176:179], v[104:107], a[168:171], v[176:179]
s_add_u32 s51, s51, 4096
buffer_load_dwordx4 v[24:27], v241, s[12:15], s51 offen offset:0 sc1
ds_read_b64_tr_b16 v[124:125], v229 offset:45056
v_mfma_f32_16x16x32_bf16 v[192:195], v[104:107], a[172:175], v[192:195]
ds_read_b64_tr_b16 v[126:127], v229 offset:45568
s_waitcnt lgkmcnt(15)
v_mfma_f32_16x16x32_bf16 v[148:151], v[108:111], a[160:163], v[148:151]
buffer_load_dwordx4 v[28:31], v241, s[12:15], s51 offen offset:2048 sc1
ds_read_b64_tr_b16 v[128:129], v230 offset:45056
v_mfma_f32_16x16x32_bf16 v[164:167], v[108:111], a[164:167], v[164:167]
ds_read_b64_tr_b16 v[130:131], v230 offset:45568
v_mfma_f32_16x16x32_bf16 v[180:183], v[108:111], a[168:171], v[180:183]
s_add_u32 s51, s51, 4096
buffer_load_dwordx4 v[32:35], v241, s[12:15], s51 offen offset:0 sc1
ds_read_b64_tr_b16 v[132:133], v231 offset:45056
s_waitcnt lgkmcnt(15)
v_mfma_f32_16x16x32_bf16 v[152:155], v[112:115], a[160:163], v[152:155]
ds_read_b64_tr_b16 v[134:135], v231 offset:45568
v_mfma_f32_16x16x32_bf16 v[168:171], v[112:115], a[164:167], v[168:171]
buffer_load_dwordx4 v[36:39], v241, s[12:15], s51 offen offset:2048 sc1
ds_read_b64_tr_b16 a[126:127], v248 offset:7168
s_add_u32 s49, s49, 31744
buffer_load_dwordx4 v244, s[24:27], s49 offen offset:2048 lds
v_mfma_f32_16x16x32_bf16 v[184:187], v[112:115], a[168:171], v[184:187]
s_waitcnt lgkmcnt(15)
v_mfma_f32_16x16x32_bf16 v[156:159], v[116:119], a[160:163], v[156:159]
s_add_u32 s49, s49, 31744
buffer_load_dwordx4 v244, s[24:27], s49 offen offset:3072 lds
v_mfma_f32_16x16x32_bf16 v[172:175], v[116:119], a[164:167], v[172:175]
v_mfma_f32_16x16x32_bf16 v[188:191], v[116:119], a[168:171], v[188:191]
s_add_u32 m0, s48, 16384
s_add_u32 s49, s49, 35840
s_cmp_eq_u32 s49, s65
s_cselect_b32 s49, s64, s49
buffer_load_dwordx4 v244, s[24:27], s49 offen offset:0 lds
s_waitcnt lgkmcnt(7)
v_mfma_f32_16x16x32_bf16 v[144:147], v[120:123], a[176:179], v[144:147]
ds_read_b64_tr_b16 v[104:105], v228 offset:49152
v_mfma_f32_16x16x32_bf16 v[160:163], v[120:123], a[180:183], v[160:163]
ds_read_b64_tr_b16 v[106:107], v228 offset:49664
v_mfma_f32_16x16x32_bf16 v[176:179], v[120:123], a[184:187], v[176:179]
ds_read_b64_tr_b16 v[108:109], v229 offset:49152
v_mfma_f32_16x16x32_bf16 v[192:195], v[120:123], a[188:191], v[192:195]
ds_read_b64_tr_b16 v[110:111], v229 offset:49664
s_waitcnt lgkmcnt(9)
v_mfma_f32_16x16x32_bf16 v[148:151], v[124:127], a[176:179], v[148:151]
ds_read_b64_tr_b16 v[112:113], v230 offset:49152
v_mfma_f32_16x16x32_bf16 v[164:167], v[124:127], a[180:183], v[164:167]
ds_read_b64_tr_b16 v[114:115], v230 offset:49664
v_mfma_f32_16x16x32_bf16 v[180:183], v[124:127], a[184:187], v[180:183]
ds_read_b64_tr_b16 v[116:117], v231 offset:49152
s_waitcnt lgkmcnt(10)
v_mfma_f32_16x16x32_bf16 v[152:155], v[128:131], a[176:179], v[152:155]
ds_read_b64_tr_b16 v[118:119], v231 offset:49664
v_mfma_f32_16x16x32_bf16 v[168:171], v[128:131], a[180:183], v[168:171]
s_add_u32 s49, s49, 31744
buffer_load_dwordx4 v244, s[24:27], s49 offen offset:1024 lds
v_mfma_f32_16x16x32_bf16 v[184:187], v[128:131], a[184:187], v[184:187]
s_waitcnt lgkmcnt(9)
v_mfma_f32_16x16x32_bf16 v[156:159], v[132:135], a[176:179], v[156:159]
s_add_u32 s49, s49, 31744
buffer_load_dwordx4 v244, s[24:27], s49 offen offset:2048 lds
v_mfma_f32_16x16x32_bf16 v[172:175], v[132:135], a[180:183], v[172:175]
v_mfma_f32_16x16x32_bf16 v[188:191], v[132:135], a[184:187], v[188:191]
s_add_u32 s49, s49, 31744
buffer_load_dwordx4 v244, s[24:27], s49 offen offset:3072 lds
s_waitcnt lgkmcnt(6)
v_mfma_f32_16x16x32_bf16 v[144:147], v[104:107], a[192:195], v[144:147]
ds_read_b64_tr_b16 v[120:121], v228 offset:53248
v_mfma_f32_16x16x32_bf16 v[160:163], v[104:107], a[196:199], v[160:163]
ds_read_b64_tr_b16 v[122:123], v228 offset:53760
v_mfma_f32_16x16x32_bf16 v[176:179], v[104:107], a[200:203], v[176:179]
ds_read_b64_tr_b16 v[124:125], v229 offset:53248
v_mfma_f32_16x16x32_bf16 v[192:195], v[104:107], a[204:207], v[192:195]
ds_read_b64_tr_b16 v[126:127], v229 offset:53760
s_waitcnt lgkmcnt(8)
v_mfma_f32_16x16x32_bf16 v[148:151], v[108:111], a[192:195], v[148:151]
ds_read_b64_tr_b16 v[128:129], v230 offset:53248
v_mfma_f32_16x16x32_bf16 v[164:167], v[108:111], a[196:199], v[164:167]
ds_read_b64_tr_b16 v[130:131], v230 offset:53760
v_mfma_f32_16x16x32_bf16 v[180:183], v[108:111], a[200:203], v[180:183]
ds_read_b64_tr_b16 v[132:133], v231 offset:53248
s_waitcnt lgkmcnt(9)
v_mfma_f32_16x16x32_bf16 v[152:155], v[112:115], a[192:195], v[152:155]
ds_read_b64_tr_b16 v[134:135], v231 offset:53760
v_mfma_f32_16x16x32_bf16 v[168:171], v[112:115], a[196:199], v[168:171]
s_waitcnt vmcnt(21)
ds_read_b64_tr_b16 a[128:129], v247 offset:8192
v_lshrrev_b32 v244, 3, v240
v_mul_u32_u24 v244, 1024, v244
v_add_u32 v244, v244, v243
s_add_u32 m0, s48, 20480
s_nop 0
buffer_load_dwordx4 v244, s[28:31], s50 offen offset:0 lds
v_mfma_f32_16x16x32_bf16 v[184:187], v[112:115], a[200:203], v[184:187]
ds_read_b64_tr_b16 a[130:131], v247 offset:9216
ds_read_b64_tr_b16 a[132:133], v248 offset:8192
s_waitcnt lgkmcnt(11)
v_mfma_f32_16x16x32_bf16 v[156:159], v[116:119], a[192:195], v[156:159]
ds_read_b64_tr_b16 a[134:135], v248 offset:9216
s_add_u32 s50, s50, 7168
buffer_load_dwordx4 v244, s[28:31], s50 offen offset:1024 lds
v_mfma_f32_16x16x32_bf16 v[172:175], v[116:119], a[196:199], v[172:175]
s_waitcnt vmcnt(21)
ds_read_b64_tr_b16 a[136:137], v247 offset:10240
ds_read_b64_tr_b16 a[138:139], v247 offset:11264
v_mfma_f32_16x16x32_bf16 v[188:191], v[116:119], a[200:203], v[188:191]
ds_read_b64_tr_b16 a[140:141], v248 offset:10240
s_add_u32 s50, s50, 7168
buffer_load_dwordx4 v244, s[28:31], s50 offen offset:2048 lds
s_waitcnt lgkmcnt(13)
v_mfma_f32_16x16x32_bf16 v[144:147], v[120:123], a[208:211], v[144:147]
ds_read_b64_tr_b16 v[104:105], v228 offset:57344
v_mfma_f32_16x16x32_bf16 v[160:163], v[120:123], a[212:215], v[160:163]
ds_read_b64_tr_b16 v[106:107], v228 offset:57856
v_mfma_f32_16x16x32_bf16 v[176:179], v[120:123], a[216:219], v[176:179]
ds_read_b64_tr_b16 v[108:109], v229 offset:57344
v_mfma_f32_16x16x32_bf16 v[192:195], v[120:123], a[220:223], v[192:195]
ds_read_b64_tr_b16 v[110:111], v229 offset:57856
s_waitcnt lgkmcnt(15)
v_mfma_f32_16x16x32_bf16 v[148:151], v[124:127], a[208:211], v[148:151]
ds_read_b64_tr_b16 v[112:113], v230 offset:57344
v_mfma_f32_16x16x32_bf16 v[164:167], v[124:127], a[212:215], v[164:167]
ds_read_b64_tr_b16 v[114:115], v230 offset:57856
v_mfma_f32_16x16x32_bf16 v[180:183], v[124:127], a[216:219], v[180:183]
ds_read_b64_tr_b16 v[116:117], v231 offset:57344
s_waitcnt lgkmcnt(15)
v_mfma_f32_16x16x32_bf16 v[152:155], v[128:131], a[208:211], v[152:155]
ds_read_b64_tr_b16 v[118:119], v231 offset:57856
v_mfma_f32_16x16x32_bf16 v[168:171], v[128:131], a[212:215], v[168:171]
ds_read_b64_tr_b16 a[142:143], v248 offset:11264
s_add_u32 s50, s50, 7168
buffer_load_dwordx4 v244, s[28:31], s50 offen offset:3072 lds
v_mfma_f32_16x16x32_bf16 v[184:187], v[128:131], a[216:219], v[184:187]
s_waitcnt vmcnt(21)
ds_read_b64_tr_b16 a[144:145], v247 offset:12288
ds_read_b64_tr_b16 a[146:147], v247 offset:13312
s_waitcnt lgkmcnt(15)
v_mfma_f32_16x16x32_bf16 v[156:159], v[132:135], a[208:211], v[156:159]
ds_read_b64_tr_b16 a[148:149], v248 offset:12288
s_add_u32 m0, s48, 0
s_add_u32 s50, s50, 11264
s_cmp_eq_u32 s50, s1
s_cselect_b32 s50, s0, s50
buffer_load_dwordx4 v244, s[28:31], s50 offen offset:0 lds
v_mfma_f32_16x16x32_bf16 v[172:175], v[132:135], a[212:215], v[172:175]
ds_read_b64_tr_b16 a[150:151], v248 offset:13312
v_mfma_f32_16x16x32_bf16 v[188:191], v[132:135], a[216:219], v[188:191]
s_add_u32 s50, s50, 7168
buffer_load_dwordx4 v244, s[28:31], s50 offen offset:1024 lds
s_waitcnt lgkmcnt(11)
v_mfma_f32_16x16x32_bf16 v[144:147], v[104:107], a[224:227], v[144:147]
ds_read_b64_tr_b16 v[120:121], v228 offset:61440
s_waitcnt vmcnt(22)
v_xor_b32 v52, s46, v41
v_mfma_f32_16x16x32_bf16 v[160:163], v[104:107], a[228:231], v[160:163]
ds_read_b64_tr_b16 v[122:123], v228 offset:61952
v_xor_b32 v53, s47, v43
v_mfma_f32_16x16x32_bf16 v[176:179], v[104:107], a[232:235], v[176:179]
ds_read_b64_tr_b16 v[124:125], v229 offset:61440
s_waitcnt vmcnt(21)
v_bitop3_b32 v52, v52, v1, s46 bitop3:0xf6
v_mfma_f32_16x16x32_bf16 v[192:195], v[104:107], a[236:239], v[192:195]
ds_read_b64_tr_b16 v[126:127], v229 offset:61952
v_bitop3_b32 v53, v53, v3, s47 bitop3:0xf6
s_waitcnt lgkmcnt(13)
v_mfma_f32_16x16x32_bf16 v[148:151], v[108:111], a[224:227], v[148:151]
ds_read_b64_tr_b16 v[128:129], v230 offset:61440
s_waitcnt vmcnt(20)
v_bitop3_b32 v52, v52, v5, s46 bitop3:0xf6
v_mfma_f32_16x16x32_bf16 v[164:167], v[108:111], a[228:231], v[164:167]
ds_read_b64_tr_b16 v[130:131], v230 offset:61952
v_bitop3_b32 v53, v53, v7, s47 bitop3:0xf6
v_mfma_f32_16x16x32_bf16 v[180:183], v[108:111], a[232:235], v[180:183]
ds_read_b64_tr_b16 v[132:133], v231 offset:61440
s_waitcnt vmcnt(19)
v_bitop3_b32 v52, v52, v9, s46 bitop3:0xf6
s_waitcnt lgkmcnt(14)
v_mfma_f32_16x16x32_bf16 v[152:155], v[112:115], a[224:227], v[152:155]
ds_read_b64_tr_b16 v[134:135], v231 offset:61952
v_bitop3_b32 v53, v53, v11, s47 bitop3:0xf6
v_mfma_f32_16x16x32_bf16 v[168:171], v[112:115], a[228:231], v[168:171]
s_waitcnt vmcnt(18)
v_bitop3_b32 v52, v52, v13, s46 bitop3:0xf6
v_bitop3_b32 v53, v53, v15, s47 bitop3:0xf6
v_mfma_f32_16x16x32_bf16 v[184:187], v[112:115], a[232:235], v[184:187]
s_waitcnt vmcnt(17)
v_bitop3_b32 v52, v52, v17, s46 bitop3:0xf6
v_bitop3_b32 v53, v53, v19, s47 bitop3:0xf6
s_waitcnt lgkmcnt(13)
v_mfma_f32_16x16x32_bf16 v[156:159], v[116:119], a[224:227], v[156:159]
s_waitcnt vmcnt(16)
v_bitop3_b32 v52, v52, v21, s46 bitop3:0xf6
v_bitop3_b32 v53, v53, v23, s47 bitop3:0xf6
v_mfma_f32_16x16x32_bf16 v[172:175], v[116:119], a[228:231], v[172:175]
s_waitcnt vmcnt(15)
v_bitop3_b32 v52, v52, v25, s46 bitop3:0xf6
v_bitop3_b32 v53, v53, v27, s47 bitop3:0xf6
v_mfma_f32_16x16x32_bf16 v[188:191], v[116:119], a[232:235], v[188:191]
s_waitcnt vmcnt(14)
v_bitop3_b32 v52, v52, v29, s46 bitop3:0xf6
v_bitop3_b32 v53, v53, v31, s47 bitop3:0xf6
s_waitcnt lgkmcnt(6)
v_mfma_f32_16x16x32_bf16 v[144:147], v[120:123], a[240:243], v[144:147]
s_waitcnt vmcnt(13)
v_bitop3_b32 v52, v52, v33, s46 bitop3:0xf6
v_bitop3_b32 v53, v53, v35, s47 bitop3:0xf6
v_mfma_f32_16x16x32_bf16 v[160:163], v[120:123], a[244:247], v[160:163]
s_waitcnt vmcnt(12)
v_bitop3_b32 v52, v52, v37, s46 bitop3:0xf6
v_bitop3_b32 v53, v53, v39, s47 bitop3:0xf6
v_mfma_f32_16x16x32_bf16 v[176:179], v[120:123], a[248:251], v[176:179]
v_cmp_ne_u64 vcc, 0, v[52:53]
s_mov_b32 s54, 0
s_nop 0
s_cbranch_vccz KVD_6
KVL_5:
s_lshl_b32 s51, s63, 11
s_add_u32 s51, s51, 262144
buffer_load_dwordx4 v[40:43], v239, s[12:15], s51 offen offset:1024 sc1
buffer_load_dwordx4 v[0:3], v241, s[12:15], s51 offen offset:0 sc1
buffer_load_dwordx4 v[4:7], v241, s[12:15], s51 offen offset:2048 sc1
s_add_u32 s51, s51, 4096
buffer_load_dwordx4 v[8:11], v241, s[12:15], s51 offen offset:0 sc1
buffer_load_dwordx4 v[12:15], v241, s[12:15], s51 offen offset:2048 sc1
s_add_u32 s51, s51, 4096
buffer_load_dwordx4 v[16:19], v241, s[12:15], s51 offen offset:0 sc1
buffer_load_dwordx4 v[20:23], v241, s[12:15], s51 offen offset:2048 sc1
s_add_u32 s51, s51, 4096
buffer_load_dwordx4 v[24:27], v241, s[12:15], s51 offen offset:0 sc1
buffer_load_dwordx4 v[28:31], v241, s[12:15], s51 offen offset:2048 sc1
s_add_u32 s51, s51, 4096
buffer_load_dwordx4 v[32:35], v241, s[12:15], s51 offen offset:0 sc1
buffer_load_dwordx4 v[36:39], v241, s[12:15], s51 offen offset:2048 sc1
s_waitcnt vmcnt(0)
v_xor_b32 v52, s46, v41
v_xor_b32 v53, s47, v43
v_bitop3_b32 v52, v52, v1, s46 bitop3:0xf6
v_bitop3_b32 v53, v53, v3, s47 bitop3:0xf6
v_bitop3_b32 v52, v52, v5, s46 bitop3:0xf6
v_bitop3_b32 v53, v53, v7, s47 bitop3:0xf6
v_bitop3_b32 v52, v52, v9, s46 bitop3:0xf6
v_bitop3_b32 v53, v53, v11, s47 bitop3:0xf6
v_bitop3_b32 v52, v52, v13, s46 bitop3:0xf6
v_bitop3_b32 v53, v53, v15, s47 bitop3:0xf6
v_bitop3_b32 v52, v52, v17, s46 bitop3:0xf6
v_bitop3_b32 v53, v53, v19, s47 bitop3:0xf6
v_bitop3_b32 v52, v52, v21, s46 bitop3:0xf6
v_bitop3_b32 v53, v53, v23, s47 bitop3:0xf6
v_bitop3_b32 v52, v52, v25, s46 bitop3:0xf6
v_bitop3_b32 v53, v53, v27, s47 bitop3:0xf6
v_bitop3_b32 v52, v52, v29, s46 bitop3:0xf6
v_bitop3_b32 v53, v53, v31, s47 bitop3:0xf6
v_bitop3_b32 v52, v52, v33, s46 bitop3:0xf6
v_bitop3_b32 v53, v53, v35, s47 bitop3:0xf6
v_bitop3_b32 v52, v52, v37, s46 bitop3:0xf6
v_bitop3_b32 v53, v53, v39, s47 bitop3:0xf6
v_cmp_ne_u64 vcc, 0, v[52:53]
s_nop 1
s_cbranch_vccz KVD_6
s_add_u32 s54, s54, 1
s_cmp_lt_u32 s54, 200000
s_cbranch_scc1 KVL_5
KVD_6:
v_and_b32 v40, v40, v216
v_mfma_f32_16x16x32_bf16 v[192:195], v[120:123], a[252:255], v[192:195]
v_and_b32 v42, v42, v216
v_lshlrev_b32 v44, 16, v0
s_waitcnt lgkmcnt(4)
v_mfma_f32_16x16x32_bf16 v[148:151], v[124:127], a[240:243], v[148:151]
v_and_b32 v45, 0xffff0000, v0
v_lshlrev_b32 v46, 16, v2
v_mfma_f32_16x16x32_bf16 v[164:167], v[124:127], a[244:247], v[164:167]
v_and_b32 v47, 0xffff0000, v2
v_lshlrev_b32 v48, 16, v40
v_mfma_f32_16x16x32_bf16 v[180:183], v[124:127], a[248:251], v[180:183]
v_and_b32 v49, 0xffff0000, v40
v_lshlrev_b32 v50, 16, v42
s_waitcnt lgkmcnt(2)
v_mfma_f32_16x16x32_bf16 v[152:155], v[128:131], a[240:243], v[152:155]
v_and_b32 v51, 0xffff0000, v42
v_dot2c_f32_bf16 v44, s52, v4
v_mfma_f32_16x16x32_bf16 v[168:171], v[128:131], a[244:247], v[168:171]
v_dot2c_f32_bf16 v45, s67, v4
v_dot2c_f32_bf16 v46, s52, v6
v_mfma_f32_16x16x32_bf16 v[184:187], v[128:131], a[248:251], v[184:187]
v_dot2c_f32_bf16 v47, s67, v6
v_dot2c_f32_bf16 v44, s52, v8
s_waitcnt lgkmcnt(0)
v_mfma_f32_16x16x32_bf16 v[156:159], v[132:135], a[240:243], v[156:159]
v_dot2c_f32_bf16 v45, s67, v8
v_dot2c_f32_bf16 v46, s52, v10
v_mfma_f32_16x16x32_bf16 v[172:175], v[132:135], a[244:247], v[172:175]
v_dot2c_f32_bf16 v47, s67, v10
v_dot2c_f32_bf16 v44, s52, v12
v_mfma_f32_16x16x32_bf16 v[188:191], v[132:135], a[248:251], v[188:191]
v_dot2c_f32_bf16 v45, s67, v12
v_dot2c_f32_bf16 v46, s52, v14
v_dot2c_f32_bf16 v47, s67, v14
v_dot2c_f32_bf16 v44, s52, v16
v_dot2c_f32_bf16 v45, s67, v16
v_dot2c_f32_bf16 v46, s52, v18
v_dot2c_f32_bf16 v47, s67, v18
v_dot2c_f32_bf16 v44, s52, v20
v_dot2c_f32_bf16 v45, s67, v20
v_dot2c_f32_bf16 v46, s52, v22
v_dot2c_f32_bf16 v47, s67, v22
v_dot2c_f32_bf16 v44, s52, v24
v_dot2c_f32_bf16 v45, s67, v24
v_dot2c_f32_bf16 v46, s52, v26
v_dot2c_f32_bf16 v47, s67, v26
v_add_f32_dpp v48, v48, v48 row_ror:1 row_mask:0xf bank_mask:0xf
v_add_f32_dpp v49, v49, v49 row_ror:1 row_mask:0xf bank_mask:0xf
v_add_f32_dpp v50, v50, v50 row_ror:1 row_mask:0xf bank_mask:0xf
v_add_f32_dpp v51, v51, v51 row_ror:1 row_mask:0xf bank_mask:0xf
v_dot2c_f32_bf16 v44, s52, v28
v_dot2c_f32_bf16 v45, s67, v28
v_dot2c_f32_bf16 v46, s52, v30
v_dot2c_f32_bf16 v47, s67, v30
v_add_f32_dpp v48, v48, v48 row_ror:2 row_mask:0xf bank_mask:0xf
v_add_f32_dpp v49, v49, v49 row_ror:2 row_mask:0xf bank_mask:0xf
v_add_f32_dpp v50, v50, v50 row_ror:2 row_mask:0xf bank_mask:0xf
v_add_f32_dpp v51, v51, v51 row_ror:2 row_mask:0xf bank_mask:0xf
v_dot2c_f32_bf16 v44, s52, v32
v_dot2c_f32_bf16 v45, s67, v32
v_dot2c_f32_bf16 v46, s52, v34
v_dot2c_f32_bf16 v47, s67, v34
v_add_f32_dpp v48, v48, v48 row_ror:4 row_mask:0xf bank_mask:0xf
v_add_f32_dpp v49, v49, v49 row_ror:4 row_mask:0xf bank_mask:0xf
v_add_f32_dpp v50, v50, v50 row_ror:4 row_mask:0xf bank_mask:0xf
v_add_f32_dpp v51, v51, v51 row_ror:4 row_mask:0xf bank_mask:0xf
v_dot2c_f32_bf16 v44, s52, v36
v_dot2c_f32_bf16 v45, s67, v36
v_dot2c_f32_bf16 v46, s52, v38
v_dot2c_f32_bf16 v47, s67, v38
v_add_f32_dpp v48, v48, v48 row_ror:8 row_mask:0xf bank_mask:0xf
v_add_f32_dpp v49, v49, v49 row_ror:8 row_mask:0xf bank_mask:0xf
v_add_f32_dpp v50, v50, v50 row_ror:8 row_mask:0xf bank_mask:0xf
v_add_f32_dpp v51, v51, v51 row_ror:8 row_mask:0xf bank_mask:0xf
v_cvt_pk_bf16_f32 v200, v44, v45
v_cvt_pk_bf16_f32 v201, v46, v47
v_cvt_pk_bf16_f32 v202, v48, v49
v_cvt_pk_bf16_f32 v203, v50, v51
ds_write_b128 v224, v[200:203]
v_pk_fma_f32 v[136:137], v[144:145], s[60:61], s[60:61] op_sel:[0,0,1] op_sel_hi:[1,0,1]
v_pk_fma_f32 v[138:139], v[146:147], s[60:61], s[60:61] op_sel:[0,0,1] op_sel_hi:[1,0,1]
s_add_u32 s50, s50, 7168
buffer_load_dwordx4 v244, s[28:31], s50 offen offset:2048 lds
v_pk_fma_f32 v[140:141], v[148:149], s[60:61], s[60:61] op_sel:[0,0,1] op_sel_hi:[1,0,1]
v_pk_fma_f32 v[142:143], v[150:151], s[60:61], s[60:61] op_sel:[0,0,1] op_sel_hi:[1,0,1]
v_exp_f32_e64 v136, v136 clamp
v_exp_f32_e64 v137, v137 clamp
s_add_u32 s50, s50, 7168
buffer_load_dwordx4 v244, s[28:31], s50 offen offset:3072 lds
v_exp_f32_e64 v138, v138 clamp
v_exp_f32_e64 v139, v139 clamp
v_exp_f32_e64 v140, v140 clamp
v_exp_f32_e64 v141, v141 clamp
s_add_u32 m0, s48, 4096
s_add_u32 s50, s50, 11264
s_cmp_eq_u32 s50, s1
s_cselect_b32 s50, s0, s50
buffer_load_dwordx4 v244, s[28:31], s50 offen offset:0 lds
v_exp_f32_e64 v142, v142 clamp
v_exp_f32_e64 v143, v143 clamp
v_max_f32 v136, v136, v144
v_max_f32 v137, v137, v145
s_add_u32 s50, s50, 7168
buffer_load_dwordx4 v244, s[28:31], s50 offen offset:1024 lds
v_max_f32 v138, v138, v146
v_max_f32 v139, v139, v147
v_max_f32 v140, v140, v148
v_max_f32 v141, v141, v149
s_add_u32 s50, s50, 7168
buffer_load_dwordx4 v244, s[28:31], s50 offen offset:2048 lds
v_max_f32 v142, v142, v150
v_max_f32 v143, v143, v151
v_pk_add_f32 v[136:137], v[136:137], s[62:63] op_sel_hi:[1,0]
v_pk_add_f32 v[138:139], v[138:139], s[62:63] op_sel_hi:[1,0]
s_add_u32 s50, s50, 7168
buffer_load_dwordx4 v244, s[28:31], s50 offen offset:3072 lds
v_pk_add_f32 v[140:141], v[140:141], s[62:63] op_sel_hi:[1,0]
v_pk_add_f32 v[142:143], v[142:143], s[62:63] op_sel_hi:[1,0]
v_cvt_pk_bf16_f32 v104, v136, v137
v_cvt_pk_bf16_f32 v105, v138, v139
s_add_u32 m0, s48, 8192
s_add_u32 s50, s50, 11264
s_cmp_eq_u32 s50, s1
s_cselect_b32 s50, s0, s50
buffer_load_dwordx4 v244, s[28:31], s50 offen offset:0 lds
v_cvt_pk_bf16_f32 v106, v140, v141
v_cvt_pk_bf16_f32 v107, v142, v143
s_waitcnt lgkmcnt(0)
s_barrier
ds_read_b128 v[204:207], v225
ds_read_b128 v[208:211], v226
ds_read_b128 v[212:215], v227
v_pk_fma_f32 v[136:137], v[152:153], s[60:61], s[60:61] op_sel:[0,0,1] op_sel_hi:[1,0,1]
v_pk_fma_f32 v[138:139], v[154:155], s[60:61], s[60:61] op_sel:[0,0,1] op_sel_hi:[1,0,1]
s_add_u32 s50, s50, 7168
buffer_load_dwordx4 v244, s[28:31], s50 offen offset:1024 lds
v_pk_fma_f32 v[140:141], v[156:157], s[60:61], s[60:61] op_sel:[0,0,1] op_sel_hi:[1,0,1]
v_pk_fma_f32 v[142:143], v[158:159], s[60:61], s[60:61] op_sel:[0,0,1] op_sel_hi:[1,0,1]
v_exp_f32_e64 v136, v136 clamp
v_exp_f32_e64 v137, v137 clamp
s_add_u32 s50, s50, 7168
buffer_load_dwordx4 v244, s[28:31], s50 offen offset:2048 lds
v_exp_f32_e64 v138, v138 clamp
v_exp_f32_e64 v139, v139 clamp
v_exp_f32_e64 v140, v140 clamp
v_mfma_f32_16x16x16_bf16 v[4:7], v[202:203], v[104:105], 0
v_exp_f32_e64 v141, v141 clamp
s_add_u32 s50, s50, 7168
buffer_load_dwordx4 v244, s[28:31], s50 offen offset:3072 lds
v_exp_f32_e64 v142, v142 clamp
v_exp_f32_e64 v143, v143 clamp
v_mfma_f32_16x16x16_bf16 v[0:3], v[200:201], v[104:105], 0
v_max_f32 v136, v136, v152
v_max_f32 v137, v137, v153
v_max_f32 v138, v138, v154
s_waitcnt lgkmcnt(2)
v_mfma_f32_16x16x16_bf16 v[12:15], v[206:207], v[106:107], 0
v_max_f32 v139, v139, v155
v_max_f32 v140, v140, v156
v_max_f32 v141, v141, v157
v_mfma_f32_16x16x16_bf16 v[8:11], v[204:205], v[106:107], 0
v_max_f32 v142, v142, v158
v_max_f32 v143, v143, v159
v_pk_add_f32 v[136:137], v[136:137], s[62:63] op_sel_hi:[1,0]
v_pk_add_f32 v[138:139], v[138:139], s[62:63] op_sel_hi:[1,0]
v_pk_add_f32 v[140:141], v[140:141], s[62:63] op_sel_hi:[1,0]
v_pk_add_f32 v[142:143], v[142:143], s[62:63] op_sel_hi:[1,0]
v_cvt_pk_bf16_f32 v108, v136, v137
v_cvt_pk_bf16_f32 v109, v138, v139
v_cvt_pk_bf16_f32 v110, v140, v141
v_cvt_pk_bf16_f32 v111, v142, v143
s_lshl_b32 s63, s56, 13
s_add_u32 s63, s63, 4718592
v_add_f32 v4, s62, v4
v_add_f32 v12, s62, v12
v_rcp_f32 v4, v4
v_rcp_f32 v12, v12
v_mul_f32 v4, v196, v4
v_mul_f32 v12, v196, v12
v_pk_mul_f32 v[0:1], v[0:1], v[4:5] op_sel_hi:[1,0]
v_pk_mul_f32 v[2:3], v[2:3], v[4:5] op_sel_hi:[1,0]
v_pk_mul_f32 v[8:9], v[8:9], v[12:13] op_sel_hi:[1,0]
v_pk_mul_f32 v[10:11], v[10:11], v[12:13] op_sel_hi:[1,0]
v_cvt_pk_bf16_f32 v144, v0, v1
v_cvt_pk_bf16_f32 v146, v8, v9
v_cvt_pk_bf16_f32 v145, v2, v3
v_cvt_pk_bf16_f32 v147, v10, v11
v_permlane16_swap_b32 v144, v146
s_nop 0
v_permlane16_swap_b32 v145, v147
buffer_store_dwordx4 v[144:147], v232, s[12:15], s63 offen sc1
v_pk_fma_f32 v[136:137], v[160:161], s[60:61], s[60:61] op_sel:[0,0,1] op_sel_hi:[1,0,1]
v_pk_fma_f32 v[138:139], v[162:163], s[60:61], s[60:61] op_sel:[0,0,1] op_sel_hi:[1,0,1]
v_pk_fma_f32 v[140:141], v[164:165], s[60:61], s[60:61] op_sel:[0,0,1] op_sel_hi:[1,0,1]
s_waitcnt lgkmcnt(1)
v_mfma_f32_16x16x16_bf16 v[20:23], v[210:211], v[108:109], 0
v_pk_fma_f32 v[142:143], v[166:167], s[60:61], s[60:61] op_sel:[0,0,1] op_sel_hi:[1,0,1]
v_exp_f32_e64 v136, v136 clamp
v_exp_f32_e64 v137, v137 clamp
v_mfma_f32_16x16x16_bf16 v[16:19], v[208:209], v[108:109], 0
v_exp_f32_e64 v138, v138 clamp
v_exp_f32_e64 v139, v139 clamp
v_exp_f32_e64 v140, v140 clamp
s_waitcnt lgkmcnt(0)
v_mfma_f32_16x16x16_bf16 v[28:31], v[214:215], v[110:111], 0
v_exp_f32_e64 v141, v141 clamp
v_exp_f32_e64 v142, v142 clamp
v_exp_f32_e64 v143, v143 clamp
v_mfma_f32_16x16x16_bf16 v[24:27], v[212:213], v[110:111], 0
v_max_f32 v136, v136, v160
v_max_f32 v137, v137, v161
v_max_f32 v138, v138, v162
v_max_f32 v139, v139, v163
v_max_f32 v140, v140, v164
v_max_f32 v141, v141, v165
v_max_f32 v142, v142, v166
v_max_f32 v143, v143, v167
v_pk_add_f32 v[136:137], v[136:137], s[62:63] op_sel_hi:[1,0]
v_pk_add_f32 v[138:139], v[138:139], s[62:63] op_sel_hi:[1,0]
v_pk_add_f32 v[140:141], v[140:141], s[62:63] op_sel_hi:[1,0]
v_pk_add_f32 v[142:143], v[142:143], s[62:63] op_sel_hi:[1,0]
v_cvt_pk_bf16_f32 v112, v136, v137
v_cvt_pk_bf16_f32 v113, v138, v139
v_cvt_pk_bf16_f32 v114, v140, v141
v_cvt_pk_bf16_f32 v115, v142, v143
v_add_f32 v20, s62, v20
v_add_f32 v28, s62, v28
v_rcp_f32 v20, v20
v_rcp_f32 v28, v28
v_mul_f32 v20, v196, v20
v_mul_f32 v28, v196, v28
v_pk_mul_f32 v[16:17], v[16:17], v[20:21] op_sel_hi:[1,0]
v_pk_mul_f32 v[18:19], v[18:19], v[20:21] op_sel_hi:[1,0]
v_pk_mul_f32 v[24:25], v[24:25], v[28:29] op_sel_hi:[1,0]
v_pk_mul_f32 v[26:27], v[26:27], v[28:29] op_sel_hi:[1,0]
v_cvt_pk_bf16_f32 v152, v16, v17
v_cvt_pk_bf16_f32 v154, v24, v25
v_cvt_pk_bf16_f32 v153, v18, v19
v_cvt_pk_bf16_f32 v155, v26, v27
v_permlane16_swap_b32 v152, v154
s_nop 0
v_permlane16_swap_b32 v153, v155
buffer_store_dwordx4 v[152:155], v233, s[12:15], s63 offen sc1
buffer_load_dwordx2 v[250:251], v222, s[12:15], 0 offen sc1
v_pk_fma_f32 v[136:137], v[168:169], s[60:61], s[60:61] op_sel:[0,0,1] op_sel_hi:[1,0,1]
v_pk_fma_f32 v[138:139], v[170:171], s[60:61], s[60:61] op_sel:[0,0,1] op_sel_hi:[1,0,1]
v_pk_fma_f32 v[140:141], v[172:173], s[60:61], s[60:61] op_sel:[0,0,1] op_sel_hi:[1,0,1]
v_mfma_f32_16x16x16_bf16 v[36:39], v[202:203], v[112:113], 0
v_pk_fma_f32 v[142:143], v[174:175], s[60:61], s[60:61] op_sel:[0,0,1] op_sel_hi:[1,0,1]
v_exp_f32_e64 v136, v136 clamp
v_exp_f32_e64 v137, v137 clamp
v_mfma_f32_16x16x16_bf16 v[32:35], v[200:201], v[112:113], 0
v_exp_f32_e64 v138, v138 clamp
v_exp_f32_e64 v139, v139 clamp
v_exp_f32_e64 v140, v140 clamp
v_mfma_f32_16x16x16_bf16 v[44:47], v[206:207], v[114:115], 0
v_exp_f32_e64 v141, v141 clamp
v_exp_f32_e64 v142, v142 clamp
v_exp_f32_e64 v143, v143 clamp
v_mfma_f32_16x16x16_bf16 v[40:43], v[204:205], v[114:115], 0
v_max_f32 v136, v136, v168
v_max_f32 v137, v137, v169
v_max_f32 v138, v138, v170
v_max_f32 v139, v139, v171
v_max_f32 v140, v140, v172
v_max_f32 v141, v141, v173
v_max_f32 v142, v142, v174
v_max_f32 v143, v143, v175
v_pk_add_f32 v[136:137], v[136:137], s[62:63] op_sel_hi:[1,0]
v_pk_add_f32 v[138:139], v[138:139], s[62:63] op_sel_hi:[1,0]
v_pk_add_f32 v[140:141], v[140:141], s[62:63] op_sel_hi:[1,0]
v_pk_add_f32 v[142:143], v[142:143], s[62:63] op_sel_hi:[1,0]
v_cvt_pk_bf16_f32 v116, v136, v137
v_cvt_pk_bf16_f32 v117, v138, v139
v_cvt_pk_bf16_f32 v118, v140, v141
v_cvt_pk_bf16_f32 v119, v142, v143
v_add_f32 v36, s62, v36
v_add_f32 v44, s62, v44
v_rcp_f32 v36, v36
v_rcp_f32 v44, v44
v_mul_f32 v36, v197, v36
v_mul_f32 v44, v197, v44
v_pk_mul_f32 v[32:33], v[32:33], v[36:37] op_sel_hi:[1,0]
v_pk_mul_f32 v[34:35], v[34:35], v[36:37] op_sel_hi:[1,0]
v_pk_mul_f32 v[40:41], v[40:41], v[44:45] op_sel_hi:[1,0]
v_pk_mul_f32 v[42:43], v[42:43], v[44:45] op_sel_hi:[1,0]
v_cvt_pk_bf16_f32 v160, v32, v33
v_cvt_pk_bf16_f32 v162, v40, v41
v_cvt_pk_bf16_f32 v161, v34, v35
v_cvt_pk_bf16_f32 v163, v42, v43
v_permlane16_swap_b32 v160, v162
s_nop 0
v_permlane16_swap_b32 v161, v163
buffer_store_dwordx4 v[160:163], v234, s[12:15], s63 offen sc1
v_pk_fma_f32 v[136:137], v[176:177], s[60:61], s[60:61] op_sel:[0,0,1] op_sel_hi:[1,0,1]
v_pk_fma_f32 v[138:139], v[178:179], s[60:61], s[60:61] op_sel:[0,0,1] op_sel_hi:[1,0,1]
v_pk_fma_f32 v[140:141], v[180:181], s[60:61], s[60:61] op_sel:[0,0,1] op_sel_hi:[1,0,1]
v_mfma_f32_16x16x16_bf16 v[52:55], v[210:211], v[116:117], 0
v_pk_fma_f32 v[142:143], v[182:183], s[60:61], s[60:61] op_sel:[0,0,1] op_sel_hi:[1,0,1]
v_exp_f32_e64 v136, v136 clamp
v_exp_f32_e64 v137, v137 clamp
v_mfma_f32_16x16x16_bf16 v[48:51], v[208:209], v[116:117], 0
v_exp_f32_e64 v138, v138 clamp
v_exp_f32_e64 v139, v139 clamp
v_exp_f32_e64 v140, v140 clamp
v_mfma_f32_16x16x16_bf16 v[60:63], v[214:215], v[118:119], 0
v_exp_f32_e64 v141, v141 clamp
v_exp_f32_e64 v142, v142 clamp
v_exp_f32_e64 v143, v143 clamp
v_mfma_f32_16x16x16_bf16 v[56:59], v[212:213], v[118:119], 0
v_max_f32 v136, v136, v176
v_max_f32 v137, v137, v177
v_max_f32 v138, v138, v178
v_max_f32 v139, v139, v179
v_max_f32 v140, v140, v180
v_max_f32 v141, v141, v181
v_max_f32 v142, v142, v182
v_max_f32 v143, v143, v183
v_pk_add_f32 v[136:137], v[136:137], s[62:63] op_sel_hi:[1,0]
v_pk_add_f32 v[138:139], v[138:139], s[62:63] op_sel_hi:[1,0]
v_pk_add_f32 v[140:141], v[140:141], s[62:63] op_sel_hi:[1,0]
v_pk_add_f32 v[142:143], v[142:143], s[62:63] op_sel_hi:[1,0]
v_cvt_pk_bf16_f32 v120, v136, v137
v_cvt_pk_bf16_f32 v121, v138, v139
v_cvt_pk_bf16_f32 v122, v140, v141
v_cvt_pk_bf16_f32 v123, v142, v143
v_add_f32 v52, s62, v52
v_add_f32 v60, s62, v60
v_rcp_f32 v52, v52
v_rcp_f32 v60, v60
v_mul_f32 v52, v197, v52
v_mul_f32 v60, v197, v60
v_pk_mul_f32 v[48:49], v[48:49], v[52:53] op_sel_hi:[1,0]
v_pk_mul_f32 v[50:51], v[50:51], v[52:53] op_sel_hi:[1,0]
v_pk_mul_f32 v[56:57], v[56:57], v[60:61] op_sel_hi:[1,0]
v_pk_mul_f32 v[58:59], v[58:59], v[60:61] op_sel_hi:[1,0]
v_cvt_pk_bf16_f32 v168, v48, v49
v_cvt_pk_bf16_f32 v170, v56, v57
v_cvt_pk_bf16_f32 v169, v50, v51
v_cvt_pk_bf16_f32 v171, v58, v59
v_permlane16_swap_b32 v168, v170
s_nop 0
v_permlane16_swap_b32 v169, v171
buffer_store_dwordx4 v[168:171], v235, s[12:15], s63 offen sc1
v_pk_fma_f32 v[136:137], v[184:185], s[60:61], s[60:61] op_sel:[0,0,1] op_sel_hi:[1,0,1]
v_pk_fma_f32 v[138:139], v[186:187], s[60:61], s[60:61] op_sel:[0,0,1] op_sel_hi:[1,0,1]
v_pk_fma_f32 v[140:141], v[188:189], s[60:61], s[60:61] op_sel:[0,0,1] op_sel_hi:[1,0,1]
v_mfma_f32_16x16x16_bf16 v[68:71], v[202:203], v[120:121], 0
v_pk_fma_f32 v[142:143], v[190:191], s[60:61], s[60:61] op_sel:[0,0,1] op_sel_hi:[1,0,1]
v_exp_f32_e64 v136, v136 clamp
v_exp_f32_e64 v137, v137 clamp
v_mfma_f32_16x16x16_bf16 v[64:67], v[200:201], v[120:121], 0
v_exp_f32_e64 v138, v138 clamp
v_exp_f32_e64 v139, v139 clamp
v_exp_f32_e64 v140, v140 clamp
v_mfma_f32_16x16x16_bf16 v[76:79], v[206:207], v[122:123], 0
v_exp_f32_e64 v141, v141 clamp
v_exp_f32_e64 v142, v142 clamp
v_exp_f32_e64 v143, v143 clamp
v_mfma_f32_16x16x16_bf16 v[72:75], v[204:205], v[122:123], 0
v_max_f32 v136, v136, v184
v_max_f32 v137, v137, v185
v_max_f32 v138, v138, v186
v_max_f32 v139, v139, v187
v_max_f32 v140, v140, v188
v_max_f32 v141, v141, v189
v_max_f32 v142, v142, v190
v_max_f32 v143, v143, v191
v_pk_add_f32 v[136:137], v[136:137], s[62:63] op_sel_hi:[1,0]
v_pk_add_f32 v[138:139], v[138:139], s[62:63] op_sel_hi:[1,0]
v_pk_add_f32 v[140:141], v[140:141], s[62:63] op_sel_hi:[1,0]
v_pk_add_f32 v[142:143], v[142:143], s[62:63] op_sel_hi:[1,0]
v_cvt_pk_bf16_f32 v124, v136, v137
v_cvt_pk_bf16_f32 v125, v138, v139
v_cvt_pk_bf16_f32 v126, v140, v141
v_cvt_pk_bf16_f32 v127, v142, v143
v_add_f32 v68, s62, v68
v_add_f32 v76, s62, v76
v_rcp_f32 v68, v68
v_rcp_f32 v76, v76
v_mul_f32 v68, v198, v68
v_mul_f32 v76, v198, v76
v_pk_mul_f32 v[64:65], v[64:65], v[68:69] op_sel_hi:[1,0]
v_pk_mul_f32 v[66:67], v[66:67], v[68:69] op_sel_hi:[1,0]
v_pk_mul_f32 v[72:73], v[72:73], v[76:77] op_sel_hi:[1,0]
v_pk_mul_f32 v[74:75], v[74:75], v[76:77] op_sel_hi:[1,0]
v_cvt_pk_bf16_f32 v176, v64, v65
v_cvt_pk_bf16_f32 v178, v72, v73
v_cvt_pk_bf16_f32 v177, v66, v67
v_cvt_pk_bf16_f32 v179, v74, v75
v_permlane16_swap_b32 v176, v178
s_nop 0
v_permlane16_swap_b32 v177, v179
buffer_store_dwordx4 v[176:179], v236, s[12:15], s63 offen sc1
v_pk_fma_f32 v[136:137], v[192:193], s[60:61], s[60:61] op_sel:[0,0,1] op_sel_hi:[1,0,1]
v_pk_fma_f32 v[138:139], v[194:195], s[60:61], s[60:61] op_sel:[0,0,1] op_sel_hi:[1,0,1]
v_exp_f32_e64 v136, v136 clamp
v_mfma_f32_16x16x16_bf16 v[84:87], v[210:211], v[124:125], 0
v_exp_f32_e64 v137, v137 clamp
v_exp_f32_e64 v138, v138 clamp
v_exp_f32_e64 v139, v139 clamp
v_mfma_f32_16x16x16_bf16 v[80:83], v[208:209], v[124:125], 0
v_max_f32 v136, v136, v192
v_max_f32 v137, v137, v193
v_max_f32 v138, v138, v194
v_mfma_f32_16x16x16_bf16 v[92:95], v[214:215], v[126:127], 0
v_max_f32 v139, v139, v195
v_pk_add_f32 v[136:137], v[136:137], s[62:63] op_sel_hi:[1,0]
v_pk_add_f32 v[138:139], v[138:139], s[62:63] op_sel_hi:[1,0]
v_mfma_f32_16x16x16_bf16 v[88:91], v[212:213], v[126:127], 0
v_cvt_pk_bf16_f32 v128, v136, v137
v_cvt_pk_bf16_f32 v129, v138, v139
s_nop 1
v_mfma_f32_16x16x16_bf16 v[100:103], v[202:203], v[128:129], 0
v_mfma_f32_16x16x16_bf16 v[96:99], v[200:201], v[128:129], 0
v_add_f32 v84, s62, v84
v_add_f32 v92, s62, v92
v_rcp_f32 v84, v84
v_rcp_f32 v92, v92
v_mul_f32 v84, v198, v84
v_mul_f32 v92, v198, v92
v_pk_mul_f32 v[80:81], v[80:81], v[84:85] op_sel_hi:[1,0]
v_pk_mul_f32 v[82:83], v[82:83], v[84:85] op_sel_hi:[1,0]
v_pk_mul_f32 v[88:89], v[88:89], v[92:93] op_sel_hi:[1,0]
v_pk_mul_f32 v[90:91], v[90:91], v[92:93] op_sel_hi:[1,0]
v_cvt_pk_bf16_f32 v184, v80, v81
v_cvt_pk_bf16_f32 v186, v88, v89
v_cvt_pk_bf16_f32 v185, v82, v83
v_cvt_pk_bf16_f32 v187, v90, v91
v_permlane16_swap_b32 v184, v186
s_nop 0
v_permlane16_swap_b32 v185, v187
buffer_store_dwordx4 v[184:187], v237, s[12:15], s63 offen sc1
v_add_f32 v100, s62, v100
v_rcp_f32 v100, v100
s_nop 0
v_mul_f32 v100, v199, v100
v_pk_mul_f32 v[96:97], v[96:97], v[100:101] op_sel_hi:[1,0]
v_pk_mul_f32 v[98:99], v[98:99], v[100:101] op_sel_hi:[1,0]
v_cvt_pk_bf16_f32 v192, v96, v97
v_cvt_pk_bf16_f32 v193, v98, v99
buffer_store_dwordx2 v[192:193], v238, s[12:15], s63 offen sc1
s_mov_b32 s67, 131072
s_waitcnt vmcnt(5)
v_cmp_ne_u64 vcc, s[46:47], v[250:251]
s_mov_b32 s54, 0
s_nop 0
s_cbranch_vccz POLLD_8
POLL_7:
buffer_load_dwordx2 v[250:251], v222, s[12:15], 0 offen sc1
s_waitcnt vmcnt(0)
v_cmp_ne_u64 vcc, s[46:47], v[250:251]
s_nop 1
s_cbranch_vccz POLLD_8
s_add_u32 s54, s54, 1
s_cmp_lt_u32 s54, 200000
s_sleep 1
s_cbranch_scc1 POLL_7
POLLD_8:
buffer_load_dwordx4 v[144:147], v245, s[12:15], s59 offen offset:0 sc1
buffer_load_dwordx4 v[148:151], v246, s[12:15], s59 offen offset:0 sc1
buffer_load_dwordx4 v[152:155], v245, s[12:15], s59 offen offset:2048 sc1
buffer_load_dwordx4 v[156:159], v246, s[12:15], s59 offen offset:2048 sc1
buffer_load_dword v252, v255, s[12:15], s67 offen sc1
buffer_load_dword v253, v221, s[12:15], s67 offen sc1
s_add_u32 s59, s59, 4096
s_cmp_eq_u32 s59, s68
s_cselect_b32 s59, s66, s59
buffer_load_dwordx4 v[160:163], v245, s[12:15], s59 offen offset:0 sc1
buffer_load_dwordx4 v[164:167], v246, s[12:15], s59 offen offset:0 sc1
s_lshl_b32 s53, s55, 3
s_add_u32 s53, s53, s56
s_lshl_b32 s53, s53, 7
s_add_u32 s53, s53, 61440
v_lshlrev_b32 v0, 16, v220
v_mov_b32 v1, v0
v_pk_mov_b32 v[2:3], v[0:1], v[0:1]
v_pk_mov_b32 v[4:5], v[0:1], v[0:1]
v_pk_mov_b32 v[6:7], v[0:1], v[0:1]
v_pk_mov_b32 v[8:9], v[0:1], v[0:1]
v_pk_mov_b32 v[10:11], v[0:1], v[0:1]
v_pk_mov_b32 v[12:13], v[0:1], v[0:1]
v_pk_mov_b32 v[14:15], v[0:1], v[0:1]
ds_read_b64_tr_b16 a[152:153], v247 offset:14336
ds_read_b64_tr_b16 a[154:155], v247 offset:15360
ds_read_b64_tr_b16 a[156:157], v248 offset:14336
ds_read_b64_tr_b16 a[158:159], v248 offset:15360
ds_read_b64_tr_b16 a[160:161], v247 offset:16384
ds_read_b64_tr_b16 a[162:163], v247 offset:17408
ds_read_b64_tr_b16 a[164:165], v248 offset:16384
ds_read_b64_tr_b16 a[166:167], v248 offset:17408
ds_read_b64_tr_b16 a[168:169], v247 offset:18432
ds_read_b64_tr_b16 a[170:171], v247 offset:19456
ds_read_b64_tr_b16 a[172:173], v248 offset:18432
ds_read_b64_tr_b16 a[174:175], v248 offset:19456
s_bfe_u32 s54, s69, 0x10001
s_lshl_b32 s54, s54, 6
v_lshrrev_b32 v248, 5, v240
v_lshl_add_u32 v248, v248, 10, s48
v_xad_u32 v247, v242, s54, v248
v_xor_b32 v248, 64, v247
ds_read_b64_tr_b16 a[176:177], v247 offset:20480
ds_read_b64_tr_b16 a[178:179], v247 offset:20992
ds_read_b64_tr_b16 a[180:181], v248 offset:20480
ds_read_b64_tr_b16 a[182:183], v248 offset:20992
ds_read_b64_tr_b16 a[184:185], v247 offset:22528
ds_read_b64_tr_b16 a[186:187], v247 offset:23040
ds_read_b64_tr_b16 a[188:189], v248 offset:22528
ds_read_b64_tr_b16 a[190:191], v248 offset:23040
ds_read_b64_tr_b16 a[192:193], v247 offset:0
ds_read_b64_tr_b16 a[194:195], v247 offset:512
ds_read_b64_tr_b16 a[196:197], v248 offset:0
ds_read_b64_tr_b16 a[198:199], v248 offset:512
s_waitcnt vmcnt(8)
s_barrier
s_cmp_lg_u32 s69, 0
s_cbranch_scc1 FL_9
v_mov_b32 v250, s46
v_mov_b32 v251, s47
s_mov_b64 exec, 1
buffer_store_dwordx2 v[250:251], off, s[12:15], s53 sc1
s_mov_b64 exec, -1
FL_9:
buffer_load_dwordx4 v[168:171], v245, s[12:15], s59 offen offset:2048 sc1
buffer_load_dwordx4 v[172:175], v246, s[12:15], s59 offen offset:2048 sc1
s_waitcnt vmcnt(9)
v_mfma_f32_32x32x16_bf16 v[16:31], v[144:147], a[4:7], 0
ds_read_b64_tr_b16 a[200:201], v247 offset:2048
ds_read_b64_tr_b16 a[202:203], v247 offset:2560
s_waitcnt vmcnt(8)
v_mfma_f32_32x32x16_bf16 v[48:63], v[148:151], a[4:7], 0
s_add_u32 s59, s59, 4096
s_cmp_eq_u32 s59, s68
s_cselect_b32 s59, s66, s59
buffer_load_dwordx4 v[176:179], v245, s[12:15], s59 offen offset:0 sc1
v_mfma_f32_32x32x16_bf16 v[32:47], v[148:151], a[0:3], 0
ds_read_b64_tr_b16 a[204:205], v248 offset:2048
ds_read_b64_tr_b16 a[206:207], v248 offset:2560
v_mfma_f32_32x32x16_bf16 v[0:15], v[144:147], a[0:3], v[0:15]
buffer_load_dwordx4 v[180:183], v246, s[12:15], s59 offen offset:0 sc1
s_waitcnt vmcnt(9)
v_mfma_f32_32x32x16_bf16 v[0:15], v[152:155], a[8:11], v[0:15]
ds_read_b64_tr_b16 a[208:209], v247 offset:4096
ds_read_b64_tr_b16 a[210:211], v247 offset:4608
v_mfma_f32_32x32x16_bf16 v[16:31], v[152:155], a[12:15], v[16:31]
buffer_load_dwordx4 v[184:187], v245, s[12:15], s59 offen offset:2048 sc1
s_waitcnt vmcnt(9)
v_mfma_f32_32x32x16_bf16 v[48:63], v[156:159], a[12:15], v[48:63]
ds_read_b64_tr_b16 a[212:213], v248 offset:4096
ds_read_b64_tr_b16 a[214:215], v248 offset:4608
v_mfma_f32_32x32x16_bf16 v[32:47], v[156:159], a[8:11], v[32:47]
buffer_load_dwordx4 v[188:191], v246, s[12:15], s59 offen offset:2048 sc1
s_waitcnt vmcnt(7)
v_mfma_f32_32x32x16_bf16 v[0:15], v[160:163], a[16:19], v[0:15]
ds_read_b64_tr_b16 a[216:217], v247 offset:6144
ds_read_b64_tr_b16 a[218:219], v247 offset:6656
v_mfma_f32_32x32x16_bf16 v[16:31], v[160:163], a[20:23], v[16:31]
s_add_u32 s59, s59, 4096
s_cmp_eq_u32 s59, s68
s_cselect_b32 s59, s66, s59
buffer_load_dwordx4 v[192:195], v245, s[12:15], s59 offen offset:0 sc1
s_waitcnt vmcnt(7)
v_mfma_f32_32x32x16_bf16 v[48:63], v[164:167], a[20:23], v[48:63]
ds_read_b64_tr_b16 a[220:221], v248 offset:6144
ds_read_b64_tr_b16 a[222:223], v248 offset:6656
v_mfma_f32_32x32x16_bf16 v[32:47], v[164:167], a[16:19], v[32:47]
buffer_load_dwordx4 v[196:199], v246, s[12:15], s59 offen offset:0 sc1
s_waitcnt vmcnt(7)
v_mfma_f32_32x32x16_bf16 v[0:15], v[168:171], a[24:27], v[0:15]
ds_read_b64_tr_b16 a[224:225], v247 offset:8192
ds_read_b64_tr_b16 a[226:227], v247 offset:8704
v_mfma_f32_32x32x16_bf16 v[16:31], v[168:171], a[28:31], v[16:31]
buffer_load_dwordx4 v[200:203], v245, s[12:15], s59 offen offset:2048 sc1
s_waitcnt vmcnt(7)
v_mfma_f32_32x32x16_bf16 v[48:63], v[172:175], a[28:31], v[48:63]
ds_read_b64_tr_b16 a[228:229], v248 offset:8192
ds_read_b64_tr_b16 a[230:231], v248 offset:8704
v_mfma_f32_32x32x16_bf16 v[32:47], v[172:175], a[24:27], v[32:47]
buffer_load_dwordx4 v[204:207], v246, s[12:15], s59 offen offset:2048 sc1
s_waitcnt vmcnt(7)
v_mfma_f32_32x32x16_bf16 v[0:15], v[176:179], a[32:35], v[0:15]
ds_read_b64_tr_b16 a[232:233], v247 offset:10240
ds_read_b64_tr_b16 a[234:235], v247 offset:10752
v_mfma_f32_32x32x16_bf16 v[16:31], v[176:179], a[36:39], v[16:31]
s_add_u32 s59, s59, 4096
s_cmp_eq_u32 s59, s68
s_cselect_b32 s59, s66, s59
buffer_load_dwordx4 v[208:211], v245, s[12:15], s59 offen offset:0 sc1
s_waitcnt vmcnt(7)
v_mfma_f32_32x32x16_bf16 v[48:63], v[180:183], a[36:39], v[48:63]
ds_read_b64_tr_b16 a[236:237], v248 offset:10240
ds_read_b64_tr_b16 a[238:239], v248 offset:10752
v_mfma_f32_32x32x16_bf16 v[32:47], v[180:183], a[32:35], v[32:47]
buffer_load_dwordx4 v[212:215], v246, s[12:15], s59 offen offset:0 sc1
s_waitcnt vmcnt(7)
v_mfma_f32_32x32x16_bf16 v[0:15], v[184:187], a[40:43], v[0:15]
v_mfma_f32_32x32x16_bf16 v[16:31], v[184:187], a[44:47], v[16:31]
buffer_load_dwordx4 v[216:219], v245, s[12:15], s59 offen offset:2048 sc1
s_waitcnt vmcnt(7)
v_mfma_f32_32x32x16_bf16 v[48:63], v[188:191], a[44:47], v[48:63]
v_mfma_f32_32x32x16_bf16 v[32:47], v[188:191], a[40:43], v[32:47]
buffer_load_dwordx4 v[220:223], v246, s[12:15], s59 offen offset:2048 sc1
s_waitcnt vmcnt(7)
v_mfma_f32_32x32x16_bf16 v[0:15], v[192:195], a[48:51], v[0:15]
v_mfma_f32_32x32x16_bf16 v[16:31], v[192:195], a[52:55], v[16:31]
s_add_u32 s59, s59, 4096
s_cmp_eq_u32 s59, s68
s_cselect_b32 s59, s66, s59
buffer_load_dwordx4 v[224:227], v245, s[12:15], s59 offen offset:0 sc1
s_waitcnt vmcnt(7)
v_mfma_f32_32x32x16_bf16 v[48:63], v[196:199], a[52:55], v[48:63]
v_mfma_f32_32x32x16_bf16 v[32:47], v[196:199], a[48:51], v[32:47]
buffer_load_dwordx4 v[228:231], v246, s[12:15], s59 offen offset:0 sc1
s_waitcnt vmcnt(7)
v_mfma_f32_32x32x16_bf16 v[0:15], v[200:203], a[56:59], v[0:15]
v_mfma_f32_32x32x16_bf16 v[16:31], v[200:203], a[60:63], v[16:31]
buffer_load_dwordx4 v[232:235], v245, s[12:15], s59 offen offset:2048 sc1
s_waitcnt vmcnt(7)
v_mfma_f32_32x32x16_bf16 v[48:63], v[204:207], a[60:63], v[48:63]
v_mfma_f32_32x32x16_bf16 v[32:47], v[204:207], a[56:59], v[32:47]
buffer_load_dwordx4 v[236:239], v246, s[12:15], s59 offen offset:2048 sc1
s_waitcnt vmcnt(7)
v_mfma_f32_32x32x16_bf16 v[0:15], v[208:211], a[64:67], v[0:15]
v_mfma_f32_32x32x16_bf16 v[16:31], v[208:211], a[68:71], v[16:31]
s_add_u32 s59, s59, 4096
s_cmp_eq_u32 s59, s68
s_cselect_b32 s59, s66, s59
buffer_load_dwordx4 v[64:67], v245, s[12:15], s59 offen offset:0 sc1
s_waitcnt vmcnt(7)
v_mfma_f32_32x32x16_bf16 v[48:63], v[212:215], a[68:71], v[48:63]
v_mfma_f32_32x32x16_bf16 v[32:47], v[212:215], a[64:67], v[32:47]
buffer_load_dwordx4 v[68:71], v246, s[12:15], s59 offen offset:0 sc1
s_waitcnt vmcnt(7)
v_mfma_f32_32x32x16_bf16 v[0:15], v[216:219], a[72:75], v[0:15]
v_mfma_f32_32x32x16_bf16 v[16:31], v[216:219], a[76:79], v[16:31]
buffer_load_dwordx4 v[72:75], v245, s[12:15], s59 offen offset:2048 sc1
s_waitcnt vmcnt(7)
v_mfma_f32_32x32x16_bf16 v[48:63], v[220:223], a[76:79], v[48:63]
v_mfma_f32_32x32x16_bf16 v[32:47], v[220:223], a[72:75], v[32:47]
buffer_load_dwordx4 v[76:79], v246, s[12:15], s59 offen offset:2048 sc1
s_waitcnt vmcnt(7)
v_mfma_f32_32x32x16_bf16 v[0:15], v[224:227], a[80:83], v[0:15]
s_lshl_b32 s51, s57, 16
s_lshl_b32 s52, s69, 14
s_add_u32 s51, s51, s52
v_mfma_f32_32x32x16_bf16 v[16:31], v[224:227], a[84:87], v[16:31]
s_add_u32 s59, s59, 4096
s_cmp_eq_u32 s59, s68
s_cselect_b32 s59, s66, s59
buffer_load_dwordx4 v[80:83], v245, s[12:15], s59 offen offset:0 sc1
s_waitcnt vmcnt(7)
v_mfma_f32_32x32x16_bf16 v[48:63], v[228:231], a[84:87], v[48:63]
s_add_u32 s49, s51, 4718592
s_mov_b32 s64, s49
s_add_u32 s65, s49, 16384
v_mfma_f32_32x32x16_bf16 v[32:47], v[228:231], a[80:83], v[32:47]
buffer_load_dwordx4 v[84:87], v246, s[12:15], s59 offen offset:0 sc1
s_waitcnt vmcnt(7)
v_mfma_f32_32x32x16_bf16 v[0:15], v[232:235], a[88:91], v[0:15]
s_bfe_u32 s51, s45, 0x20008
s_mul_i32 s51, s51, 4096
s_add_u32 s49, s49, s51
v_mfma_f32_32x32x16_bf16 v[16:31], v[232:235], a[92:95], v[16:31]
buffer_load_dwordx4 v[88:91], v245, s[12:15], s59 offen offset:2048 sc1
s_waitcnt vmcnt(7)
v_mfma_f32_32x32x16_bf16 v[48:63], v[236:239], a[92:95], v[48:63]
s_bfe_u32 s51, s69, 0x10000
s_lshl_b32 s51, s51, 10
v_add_u32 v220, s51, v241
v_mfma_f32_32x32x16_bf16 v[32:47], v[236:239], a[88:91], v[32:47]
buffer_load_dwordx4 v[92:95], v246, s[12:15], s59 offen offset:2048 sc1
s_waitcnt vmcnt(7)
v_mfma_f32_32x32x16_bf16 v[0:15], v[64:67], a[96:99], v[0:15]
v_xor_b32 v221, 0x400, v220
s_xor_b32 s51, s69, 1
s_mul_i32 s51, s51, 12288
v_mfma_f32_32x32x16_bf16 v[16:31], v[64:67], a[100:103], v[16:31]
s_add_u32 s59, s59, 4096
s_cmp_eq_u32 s59, s68
s_cselect_b32 s59, s66, s59
buffer_load_dwordx4 v[96:99], v245, s[12:15], s59 offen offset:0 sc1
s_waitcnt vmcnt(7)
v_mfma_f32_32x32x16_bf16 v[48:63], v[68:71], a[100:103], v[48:63]
s_add_u32 s51, s51, 98304
v_add_u32 v217, s51, v241
s_xor_b32 s51, s69, 2
v_mfma_f32_32x32x16_bf16 v[32:47], v[68:71], a[96:99], v[32:47]
buffer_load_dwordx4 v[100:103], v246, s[12:15], s59 offen offset:0 sc1
s_waitcnt vmcnt(7)
v_mfma_f32_32x32x16_bf16 v[0:15], v[72:75], a[104:107], v[0:15]
s_mul_i32 s51, s51, 12288
s_add_u32 s51, s51, 102400
v_add_u32 v218, s51, v241
v_mfma_f32_32x32x16_bf16 v[16:31], v[72:75], a[108:111], v[16:31]
buffer_load_dwordx4 v[104:107], v245, s[12:15], s59 offen offset:2048 sc1
s_waitcnt vmcnt(7)
v_mfma_f32_32x32x16_bf16 v[48:63], v[76:79], a[108:111], v[48:63]
s_xor_b32 s51, s69, 3
s_mul_i32 s51, s51, 12288
s_add_u32 s51, s51, 106496
v_mfma_f32_32x32x16_bf16 v[32:47], v[76:79], a[104:107], v[32:47]
buffer_load_dwordx4 v[108:111], v246, s[12:15], s59 offen offset:2048 sc1
buffer_load_dwordx2 v[250:251], v249, s[12:15], 0 offen sc1
s_waitcnt vmcnt(8)
v_mfma_f32_32x32x16_bf16 v[0:15], v[80:83], a[112:115], v[0:15]
v_add_u32 v219, s51, v241
s_mul_i32 s51, s69, 12288
s_add_u32 s51, s51, 98304
v_mfma_f32_32x32x16_bf16 v[16:31], v[80:83], a[116:119], v[16:31]
s_add_u32 s59, s59, 4096
s_cmp_eq_u32 s59, s68
s_cselect_b32 s59, s66, s59
buffer_load_dwordx4 v[112:115], v245, s[12:15], s59 offen offset:0 sc1
s_waitcnt vmcnt(8)
v_mfma_f32_32x32x16_bf16 v[48:63], v[84:87], a[116:119], v[48:63]
v_add_u32 v216, s51, v241
v_sub_f32 v252, 1.0, v252
v_sub_f32 v253, 1.0, v253
v_mfma_f32_32x32x16_bf16 v[32:47], v[84:87], a[112:115], v[32:47]
buffer_load_dwordx4 v[116:119], v246, s[12:15], s59 offen offset:0 sc1
s_waitcnt vmcnt(8)
v_mfma_f32_32x32x16_bf16 v[0:15], v[88:91], a[120:123], v[0:15]
v_mfma_f32_32x32x16_bf16 v[16:31], v[88:91], a[124:127], v[16:31]
buffer_load_dwordx4 v[120:123], v245, s[12:15], s59 offen offset:2048 sc1
s_waitcnt vmcnt(8)
v_mfma_f32_32x32x16_bf16 v[48:63], v[92:95], a[124:127], v[48:63]
v_mfma_f32_32x32x16_bf16 v[32:47], v[92:95], a[120:123], v[32:47]
buffer_load_dwordx4 v[124:127], v246, s[12:15], s59 offen offset:2048 sc1
s_waitcnt vmcnt(8)
v_mfma_f32_32x32x16_bf16 v[0:15], v[96:99], a[128:131], v[0:15]
v_mfma_f32_32x32x16_bf16 v[16:31], v[96:99], a[132:135], v[16:31]
s_add_u32 s59, s59, 4096
s_cmp_eq_u32 s59, s68
s_cselect_b32 s59, s66, s59
buffer_load_dwordx4 v[128:131], v245, s[12:15], s59 offen offset:0 sc1
s_waitcnt vmcnt(8)
v_mfma_f32_32x32x16_bf16 v[48:63], v[100:103], a[132:135], v[48:63]
v_mfma_f32_32x32x16_bf16 v[32:47], v[100:103], a[128:131], v[32:47]
buffer_load_dwordx4 v[132:135], v246, s[12:15], s59 offen offset:0 sc1
s_waitcnt vmcnt(8)
v_mfma_f32_32x32x16_bf16 v[0:15], v[104:107], a[136:139], v[0:15]
v_mfma_f32_32x32x16_bf16 v[16:31], v[104:107], a[140:143], v[16:31]
buffer_load_dwordx4 v[136:139], v245, s[12:15], s59 offen offset:2048 sc1
s_waitcnt vmcnt(8)
v_mfma_f32_32x32x16_bf16 v[48:63], v[108:111], a[140:143], v[48:63]
v_mfma_f32_32x32x16_bf16 v[32:47], v[108:111], a[136:139], v[32:47]
buffer_load_dwordx4 v[140:143], v246, s[12:15], s59 offen offset:2048 sc1
s_waitcnt vmcnt(8)
v_cmp_ne_u64 vcc, s[46:47], v[250:251]
s_nop 1
s_cbranch_vccnz SLOW_10
s_waitcnt vmcnt(7)
v_mfma_f32_32x32x16_bf16 v[0:15], v[112:115], a[144:147], v[0:15]
v_mfma_f32_32x32x16_bf16 v[16:31], v[112:115], a[148:151], v[16:31]
s_mov_b32 s53, s49
buffer_load_dwordx4 v[148:151], v221, s[12:15], s49 offen offset:0 sc1
s_waitcnt vmcnt(7)
v_mfma_f32_32x32x16_bf16 v[48:63], v[116:119], a[148:151], v[48:63]
v_mfma_f32_32x32x16_bf16 v[32:47], v[116:119], a[144:147], v[32:47]
buffer_load_dwordx4 v[156:159], v221, s[12:15], s49 offen offset:2048 sc1
s_waitcnt vmcnt(7) lgkmcnt(15)
v_mfma_f32_32x32x16_bf16 v[0:15], v[120:123], a[152:155], v[0:15]
v_mfma_f32_32x32x16_bf16 v[16:31], v[120:123], a[156:159], v[16:31]
s_add_u32 s49, s49, 4096
s_cmp_eq_u32 s49, s65
s_cselect_b32 s49, s64, s49
buffer_load_dwordx4 v[164:167], v221, s[12:15], s49 offen offset:0 sc1
s_waitcnt vmcnt(7)
v_mfma_f32_32x32x16_bf16 v[48:63], v[124:127], a[156:159], v[48:63]
v_mfma_f32_32x32x16_bf16 v[32:47], v[124:127], a[152:155], v[32:47]
buffer_load_dwordx4 v[172:175], v221, s[12:15], s49 offen offset:2048 sc1
s_waitcnt vmcnt(7)
v_mfma_f32_32x32x16_bf16 v[0:15], v[128:131], a[160:163], v[0:15]
v_mfma_f32_32x32x16_bf16 v[16:31], v[128:131], a[164:167], v[16:31]
s_add_u32 s49, s49, 4096
s_cmp_eq_u32 s49, s65
s_cselect_b32 s49, s64, s49
buffer_load_dwordx4 v[180:183], v221, s[12:15], s49 offen offset:0 sc1
s_waitcnt vmcnt(7)
v_mfma_f32_32x32x16_bf16 v[48:63], v[132:135], a[164:167], v[48:63]
v_mfma_f32_32x32x16_bf16 v[32:47], v[132:135], a[160:163], v[32:47]
buffer_load_dwordx4 v[188:191], v221, s[12:15], s49 offen offset:2048 sc1
s_waitcnt vmcnt(7)
v_mfma_f32_32x32x16_bf16 v[0:15], v[136:139], a[168:171], v[0:15]
v_mfma_f32_32x32x16_bf16 v[16:31], v[136:139], a[172:175], v[16:31]
s_add_u32 s49, s49, 4096
s_cmp_eq_u32 s49, s65
s_cselect_b32 s49, s64, s49
buffer_load_dwordx4 v[196:199], v221, s[12:15], s49 offen offset:0 sc1
s_waitcnt vmcnt(7)
v_mfma_f32_32x32x16_bf16 v[48:63], v[140:143], a[172:175], v[48:63]
v_mfma_f32_32x32x16_bf16 v[32:47], v[140:143], a[168:171], v[32:47]
buffer_load_dwordx4 v[204:207], v221, s[12:15], s49 offen offset:2048 sc1
JOIN_11:
s_nop 2
v_pk_mul_f32 v[0:1], v[0:1], v[252:253] op_sel_hi:[1,0]
v_pk_mul_f32 v[2:3], v[2:3], v[252:253] op_sel_hi:[1,0]
v_pk_mul_f32 v[4:5], v[4:5], v[252:253] op_sel_hi:[1,0]
v_pk_mul_f32 v[6:7], v[6:7], v[252:253] op_sel_hi:[1,0]
v_pk_mul_f32 v[8:9], v[8:9], v[252:253] op_sel_hi:[1,0]
v_pk_mul_f32 v[10:11], v[10:11], v[252:253] op_sel_hi:[1,0]
v_pk_mul_f32 v[12:13], v[12:13], v[252:253] op_sel_hi:[1,0]
v_pk_mul_f32 v[14:15], v[14:15], v[252:253] op_sel_hi:[1,0]
v_pk_mul_f32 v[16:17], v[16:17], v[252:253] op_sel:[0,1] op_sel_hi:[1,1]
v_pk_mul_f32 v[18:19], v[18:19], v[252:253] op_sel:[0,1] op_sel_hi:[1,1]
v_pk_mul_f32 v[20:21], v[20:21], v[252:253] op_sel:[0,1] op_sel_hi:[1,1]
v_pk_mul_f32 v[22:23], v[22:23], v[252:253] op_sel:[0,1] op_sel_hi:[1,1]
v_pk_mul_f32 v[24:25], v[24:25], v[252:253] op_sel:[0,1] op_sel_hi:[1,1]
v_pk_mul_f32 v[26:27], v[26:27], v[252:253] op_sel:[0,1] op_sel_hi:[1,1]
v_pk_mul_f32 v[28:29], v[28:29], v[252:253] op_sel:[0,1] op_sel_hi:[1,1]
v_pk_mul_f32 v[30:31], v[30:31], v[252:253] op_sel:[0,1] op_sel_hi:[1,1]
v_pk_mul_f32 v[32:33], v[32:33], v[252:253] op_sel_hi:[1,0]
v_pk_mul_f32 v[34:35], v[34:35], v[252:253] op_sel_hi:[1,0]
v_pk_mul_f32 v[36:37], v[36:37], v[252:253] op_sel_hi:[1,0]
v_pk_mul_f32 v[38:39], v[38:39], v[252:253] op_sel_hi:[1,0]
v_pk_mul_f32 v[40:41], v[40:41], v[252:253] op_sel_hi:[1,0]
v_pk_mul_f32 v[42:43], v[42:43], v[252:253] op_sel_hi:[1,0]
v_pk_mul_f32 v[44:45], v[44:45], v[252:253] op_sel_hi:[1,0]
v_pk_mul_f32 v[46:47], v[46:47], v[252:253] op_sel_hi:[1,0]
v_pk_mul_f32 v[48:49], v[48:49], v[252:253] op_sel:[0,1] op_sel_hi:[1,1]
v_pk_mul_f32 v[50:51], v[50:51], v[252:253] op_sel:[0,1] op_sel_hi:[1,1]
v_pk_mul_f32 v[52:53], v[52:53], v[252:253] op_sel:[0,1] op_sel_hi:[1,1]
v_pk_mul_f32 v[54:55], v[54:55], v[252:253] op_sel:[0,1] op_sel_hi:[1,1]
v_pk_mul_f32 v[56:57], v[56:57], v[252:253] op_sel:[0,1] op_sel_hi:[1,1]
v_pk_mul_f32 v[58:59], v[58:59], v[252:253] op_sel:[0,1] op_sel_hi:[1,1]
v_pk_mul_f32 v[60:61], v[60:61], v[252:253] op_sel:[0,1] op_sel_hi:[1,1]
v_pk_mul_f32 v[62:63], v[62:63], v[252:253] op_sel:[0,1] op_sel_hi:[1,1]
s_waitcnt vmcnt(7)
s_nop 0
v_mfma_f32_32x32x16_bf16 v[48:63], a[180:183], v[148:151], v[48:63]
v_mfma_f32_32x32x16_bf16 v[16:31], a[176:179], v[148:151], v[16:31]
s_mov_b32 s49, s53
buffer_load_dwordx4 v[144:147], v220, s[12:15], s49 offen offset:0 sc1
s_waitcnt vmcnt(7)
v_mfma_f32_32x32x16_bf16 v[16:31], a[184:187], v[156:159], v[16:31]
v_mfma_f32_32x32x16_bf16 v[48:63], a[188:191], v[156:159], v[48:63]
buffer_load_dwordx4 v[152:155], v220, s[12:15], s49 offen offset:2048 sc1
s_waitcnt vmcnt(7)
v_mfma_f32_32x32x16_bf16 v[48:63], a[196:199], v[164:167], v[48:63]
v_mfma_f32_32x32x16_bf16 v[16:31], a[192:195], v[164:167], v[16:31]
s_add_u32 s49, s49, 4096
s_cmp_eq_u32 s49, s65
s_cselect_b32 s49, s64, s49
buffer_load_dwordx4 v[160:163], v220, s[12:15], s49 offen offset:0 sc1
s_waitcnt vmcnt(7)
v_mfma_f32_32x32x16_bf16 v[16:31], a[200:203], v[172:175], v[16:31]
v_mfma_f32_32x32x16_bf16 v[48:63], a[204:207], v[172:175], v[48:63]
buffer_load_dwordx4 v[168:171], v220, s[12:15], s49 offen offset:2048 sc1
s_waitcnt vmcnt(7) lgkmcnt(12)
v_mfma_f32_32x32x16_bf16 v[48:63], a[212:215], v[180:183], v[48:63]
v_mfma_f32_32x32x16_bf16 v[16:31], a[208:211], v[180:183], v[16:31]
s_add_u32 s49, s49, 4096
s_cmp_eq_u32 s49, s65
s_cselect_b32 s49, s64, s49
buffer_load_dwordx4 v[176:179], v220, s[12:15], s49 offen offset:0 sc1
s_waitcnt vmcnt(7) lgkmcnt(10)
v_mfma_f32_32x32x16_bf16 v[16:31], a[216:219], v[188:191], v[16:31]
s_waitcnt lgkmcnt(8)
v_mfma_f32_32x32x16_bf16 v[48:63], a[220:223], v[188:191], v[48:63]
buffer_load_dwordx4 v[184:187], v220, s[12:15], s49 offen offset:2048 sc1
s_waitcnt vmcnt(7) lgkmcnt(4)
v_mfma_f32_32x32x16_bf16 v[48:63], a[228:231], v[196:199], v[48:63]
v_mfma_f32_32x32x16_bf16 v[16:31], a[224:227], v[196:199], v[16:31]
s_add_u32 s49, s49, 4096
s_cmp_eq_u32 s49, s65
s_cselect_b32 s49, s64, s49
buffer_load_dwordx4 v[192:195], v220, s[12:15], s49 offen offset:0 sc1
s_waitcnt vmcnt(7) lgkmcnt(2)
v_mfma_f32_32x32x16_bf16 v[16:31], a[232:235], v[204:207], v[16:31]
s_waitcnt lgkmcnt(0)
v_mfma_f32_32x32x16_bf16 v[48:63], a[236:239], v[204:207], v[48:63]
buffer_load_dwordx4 v[200:203], v220, s[12:15], s49 offen offset:2048 sc1
s_waitcnt vmcnt(7)
v_mfma_f32_32x32x16_bf16 v[32:47], a[180:183], v[144:147], v[32:47]
s_nop 6
ds_write_b128 v217, v[16:19] offset:0
v_mfma_f32_32x32x16_bf16 v[0:15], a[176:179], v[144:147], v[0:15]
ds_write_b128 v217, v[20:23] offset:1024
s_waitcnt vmcnt(6)
v_mfma_f32_32x32x16_bf16 v[32:47], a[188:191], v[152:155], v[32:47]
ds_write_b128 v217, v[24:27] offset:2048
v_mfma_f32_32x32x16_bf16 v[0:15], a[184:187], v[152:155], v[0:15]
ds_write_b128 v217, v[28:31] offset:3072
s_waitcnt vmcnt(5)
v_mfma_f32_32x32x16_bf16 v[32:47], a[196:199], v[160:163], v[32:47]
ds_write_b128 v219, v[48:51] offset:0
v_mfma_f32_32x32x16_bf16 v[0:15], a[192:195], v[160:163], v[0:15]
ds_write_b128 v219, v[52:55] offset:1024
s_waitcnt vmcnt(4)
v_mfma_f32_32x32x16_bf16 v[32:47], a[204:207], v[168:171], v[32:47]
ds_write_b128 v219, v[56:59] offset:2048
v_mfma_f32_32x32x16_bf16 v[0:15], a[200:203], v[168:171], v[0:15]
ds_write_b128 v219, v[60:63] offset:3072
s_waitcnt vmcnt(3)
v_mfma_f32_32x32x16_bf16 v[32:47], a[212:215], v[176:179], v[32:47]
v_mfma_f32_32x32x16_bf16 v[0:15], a[208:211], v[176:179], v[0:15]
s_waitcnt vmcnt(2)
v_mfma_f32_32x32x16_bf16 v[32:47], a[220:223], v[184:187], v[32:47]
s_waitcnt vmcnt(1)
v_mfma_f32_32x32x16_bf16 v[32:47], a[228:231], v[192:195], v[32:47]
s_waitcnt vmcnt(0)
v_mfma_f32_32x32x16_bf16 v[32:47], a[236:239], v[200:203], v[32:47]
v_mfma_f32_32x32x16_bf16 v[0:15], a[216:219], v[184:187], v[0:15]
v_mfma_f32_32x32x16_bf16 v[0:15], a[224:227], v[192:195], v[0:15]
s_nop 9
ds_write_b128 v218, v[32:35] offset:0
v_mfma_f32_32x32x16_bf16 v[0:15], a[232:235], v[200:203], v[0:15]
ds_write_b128 v218, v[36:39] offset:1024
ds_write_b128 v218, v[40:43] offset:2048
ds_write_b128 v218, v[44:47] offset:3072
s_waitcnt lgkmcnt(0)
s_barrier
ds_read_b128 v[16:19], v216 offset:0
ds_read_b128 v[32:35], v216 offset:4096
ds_read_b128 v[48:51], v216 offset:8192
ds_read_b128 v[20:23], v216 offset:1024
ds_read_b128 v[36:39], v216 offset:5120
ds_read_b128 v[52:55], v216 offset:9216
ds_read_b128 v[24:27], v216 offset:2048
ds_read_b128 v[40:43], v216 offset:6144
ds_read_b128 v[56:59], v216 offset:10240
ds_read_b128 v[28:31], v216 offset:3072
ds_read_b128 v[44:47], v216 offset:7168
ds_read_b128 v[60:63], v216 offset:11264
s_lshr_b32 s51, s69, 1
s_lshl_b32 s51, s51, 6
s_lshl_b32 s52, s58, 7
s_add_u32 s51, s51, s52
v_lshrrev_b32 v255, 5, v240
v_lshlrev_b32 v255, 4, v255
v_lshl_add_u32 v255, v254, 10, v255
s_waitcnt lgkmcnt(11)
v_pk_add_f32 v[0:1], v[0:1], v[16:17]
v_pk_add_f32 v[2:3], v[2:3], v[18:19]
s_waitcnt lgkmcnt(9)
v_pk_add_f32 v[32:33], v[32:33], v[48:49]
v_pk_add_f32 v[34:35], v[34:35], v[50:51]
v_pk_add_f32 v[0:1], v[0:1], v[32:33]
v_pk_add_f32 v[2:3], v[2:3], v[34:35]
v_cvt_pk_bf16_f32 v72, v0, v1
v_cvt_pk_bf16_f32 v73, v2, v3
s_waitcnt lgkmcnt(8)
v_pk_add_f32 v[4:5], v[4:5], v[20:21]
v_pk_add_f32 v[6:7], v[6:7], v[22:23]
s_waitcnt lgkmcnt(6)
v_pk_add_f32 v[36:37], v[36:37], v[52:53]
v_pk_add_f32 v[38:39], v[38:39], v[54:55]
v_pk_add_f32 v[4:5], v[4:5], v[36:37]
v_pk_add_f32 v[6:7], v[6:7], v[38:39]
v_cvt_pk_bf16_f32 v74, v4, v5
v_cvt_pk_bf16_f32 v75, v6, v7
s_nop 0
v_permlane32_swap_b32 v72, v74
v_permlane32_swap_b32 v73, v75
buffer_store_dwordx4 v[72:75], v255, s[40:43], s51 offen offset:0
s_waitcnt lgkmcnt(5)
v_pk_add_f32 v[8:9], v[8:9], v[24:25]
v_pk_add_f32 v[10:11], v[10:11], v[26:27]
s_waitcnt lgkmcnt(3)
v_pk_add_f32 v[40:41], v[40:41], v[56:57]
v_pk_add_f32 v[42:43], v[42:43], v[58:59]
v_pk_add_f32 v[8:9], v[8:9], v[40:41]
v_pk_add_f32 v[10:11], v[10:11], v[42:43]
v_cvt_pk_bf16_f32 v76, v8, v9
v_cvt_pk_bf16_f32 v77, v10, v11
s_waitcnt lgkmcnt(2)
v_pk_add_f32 v[12:13], v[12:13], v[28:29]
v_pk_add_f32 v[14:15], v[14:15], v[30:31]
s_waitcnt lgkmcnt(0)
v_pk_add_f32 v[44:45], v[44:45], v[60:61]
v_pk_add_f32 v[46:47], v[46:47], v[62:63]
v_pk_add_f32 v[12:13], v[12:13], v[44:45]
v_pk_add_f32 v[14:15], v[14:15], v[46:47]
v_cvt_pk_bf16_f32 v78, v12, v13
v_cvt_pk_bf16_f32 v79, v14, v15
s_nop 0
v_permlane32_swap_b32 v76, v78
v_permlane32_swap_b32 v77, v79
buffer_store_dwordx4 v[76:79], v255, s[40:43], s51 offen offset:32
s_endpgm
SLOW_10:
buffer_load_dwordx2 v[250:251], v249, s[12:15], 0 offen sc1
s_waitcnt vmcnt(8)
v_mfma_f32_32x32x16_bf16 v[0:15], v[112:115], a[144:147], v[0:15]
v_mfma_f32_32x32x16_bf16 v[16:31], v[112:115], a[148:151], v[16:31]
s_waitcnt vmcnt(7)
v_mfma_f32_32x32x16_bf16 v[48:63], v[116:119], a[148:151], v[48:63]
v_mfma_f32_32x32x16_bf16 v[32:47], v[116:119], a[144:147], v[32:47]
s_waitcnt vmcnt(6) lgkmcnt(15)
v_mfma_f32_32x32x16_bf16 v[0:15], v[120:123], a[152:155], v[0:15]
v_mfma_f32_32x32x16_bf16 v[16:31], v[120:123], a[156:159], v[16:31]
s_waitcnt vmcnt(5)
v_mfma_f32_32x32x16_bf16 v[48:63], v[124:127], a[156:159], v[48:63]
v_mfma_f32_32x32x16_bf16 v[32:47], v[124:127], a[152:155], v[32:47]
s_waitcnt vmcnt(4)
v_mfma_f32_32x32x16_bf16 v[0:15], v[128:131], a[160:163], v[0:15]
v_mfma_f32_32x32x16_bf16 v[16:31], v[128:131], a[164:167], v[16:31]
s_waitcnt vmcnt(3)
v_mfma_f32_32x32x16_bf16 v[48:63], v[132:135], a[164:167], v[48:63]
v_mfma_f32_32x32x16_bf16 v[32:47], v[132:135], a[160:163], v[32:47]
s_waitcnt vmcnt(2)
v_mfma_f32_32x32x16_bf16 v[0:15], v[136:139], a[168:171], v[0:15]
v_mfma_f32_32x32x16_bf16 v[16:31], v[136:139], a[172:175], v[16:31]
s_waitcnt vmcnt(1)
v_mfma_f32_32x32x16_bf16 v[48:63], v[140:143], a[172:175], v[48:63]
v_mfma_f32_32x32x16_bf16 v[32:47], v[140:143], a[168:171], v[32:47]
s_waitcnt vmcnt(0)
v_cmp_ne_u64 vcc, s[46:47], v[250:251]
s_mov_b32 s54, 0
s_nop 0
s_cbranch_vccz POLLD_13
POLL_12:
buffer_load_dwordx2 v[250:251], v249, s[12:15], 0 offen sc1
s_waitcnt vmcnt(0)
v_cmp_ne_u64 vcc, s[46:47], v[250:251]
s_nop 1
s_cbranch_vccz POLLD_13
s_add_u32 s54, s54, 1
s_cmp_lt_u32 s54, 200000
s_sleep 1
s_cbranch_scc1 POLL_12
POLLD_13:
s_mov_b32 s53, s49
buffer_load_dwordx4 v[148:151], v221, s[12:15], s49 offen offset:0 sc1
buffer_load_dwordx4 v[156:159], v221, s[12:15], s49 offen offset:2048 sc1
s_add_u32 s49, s49, 4096
s_cmp_eq_u32 s49, s65
s_cselect_b32 s49, s64, s49
buffer_load_dwordx4 v[164:167], v221, s[12:15], s49 offen offset:0 sc1
buffer_load_dwordx4 v[172:175], v221, s[12:15], s49 offen offset:2048 sc1
s_add_u32 s49, s49, 4096
s_cmp_eq_u32 s49, s65
s_cselect_b32 s49, s64, s49
buffer_load_dwordx4 v[180:183], v221, s[12:15], s49 offen offset:0 sc1
buffer_load_dwordx4 v[188:191], v221, s[12:15], s49 offen offset:2048 sc1
s_add_u32 s49, s49, 4096
s_cmp_eq_u32 s49, s65
s_cselect_b32 s49, s64, s49
buffer_load_dwordx4 v[196:199], v221, s[12:15], s49 offen offset:0 sc1
buffer_load_dwordx4 v[204:207], v221, s[12:15], s49 offen offset:2048 sc1
s_branch JOIN_11
.p2align 7
ROLE_U:
v_and_b32 v99, 31, v0
v_lshrrev_b32 v98, 5, v0
v_lshlrev_b32 v99, 4, v99
v_lshl_add_u32 v2, v98, 10, v99
v_and_b32 v240, 63, v0
v_lshrrev_b32 v1, 2, v0
v_and_b32 v1, 4, v1
v_and_b32 v3, 7, v0
v_xor_b32 v1, v1, v3
v_lshlrev_b32 v243, 4, v1
s_mul_i32 s48, s69, 24576
v_lshrrev_b32 v244, 3, v240
v_mul_u32_u24 v244, 2752, v244
v_add_u32 v244, v244, v243
s_mul_i32 s49, s69, 1409024
s_waitcnt lgkmcnt(0)
s_and_b32 s3, s45, 2088960
s_buffer_load_dwordx2 s[46:47], s[12:15], 0x0
s_and_b32 s55, s44, 31
s_bfe_u32 s56, s44, 0x30005
s_lshl_b32 s51, s55, 7
s_add_u32 s49, s49, s51
s_mov_b32 s64, s49
s_add_u32 s65, s49, 1409024
s_bfe_u32 s51, s45, 0x40000
s_mul_i32 s51, s51, 88064
s_add_u32 s49, s49, s51
s_add_u32 m0, s48, 0
s_nop 0
buffer_load_dwordx4 v244, s[16:19], s49 offen offset:0 lds
buffer_load_dwordx4 v[64:67], v2, s[4:7], s3 offen
buffer_load_dwordx4 v[68:71], v2, s[4:7], s3 offen offset:512
buffer_load_dwordx4 v[72:75], v99, s[8:11], 0 offen
buffer_load_dwordx4 v[76:79], v99, s[8:11], 0 offen offset:512
s_add_u32 s49, s49, 20992
buffer_load_dwordx4 v244, s[16:19], s49 offen offset:1024 lds
s_add_u32 s49, s49, 20992
buffer_load_dwordx4 v244, s[16:19], s49 offen offset:2048 lds
s_add_u32 s49, s49, 20992
buffer_load_dwordx4 v244, s[16:19], s49 offen offset:3072 lds
s_add_u32 m0, s48, 4096
s_add_u32 s49, s49, 25088
s_cmp_eq_u32 s49, s65
s_cselect_b32 s49, s64, s49
buffer_load_dwordx4 v244, s[16:19], s49 offen offset:0 lds
s_add_u32 s49, s49, 20992
buffer_load_dwordx4 v244, s[16:19], s49 offen offset:1024 lds
s_add_u32 s49, s49, 20992
buffer_load_dwordx4 v244, s[16:19], s49 offen offset:2048 lds
s_add_u32 s49, s49, 20992
buffer_load_dwordx4 v244, s[16:19], s49 offen offset:3072 lds
s_add_u32 m0, s48, 8192
s_add_u32 s49, s49, 25088
s_cmp_eq_u32 s49, s65
s_cselect_b32 s49, s64, s49
buffer_load_dwordx4 v244, s[16:19], s49 offen offset:0 lds
s_add_u32 s49, s49, 20992
buffer_load_dwordx4 v244, s[16:19], s49 offen offset:1024 lds
s_add_u32 s49, s49, 20992
buffer_load_dwordx4 v244, s[16:19], s49 offen offset:2048 lds
s_add_u32 s49, s49, 20992
buffer_load_dwordx4 v244, s[16:19], s49 offen offset:3072 lds
v_lshlrev_b32 v241, 4, v240
v_bfe_u32 v1, v0, 2, 2
v_lshlrev_b32 v242, 7, v1
v_bfe_u32 v1, v0, 3, 1
v_lshl_or_b32 v242, v1, 6, v242
v_bfe_u32 v1, v0, 4, 1
v_lshl_or_b32 v242, v1, 5, v242
v_and_b32 v1, 3, v0
v_lshl_or_b32 v242, v1, 3, v242
s_lshr_b32 s63, s3, 13
s_bfe_u32 s57, s44, 0x50009
s_bfe_u32 s58, s44, 0x3000e
v_mul_u32_u24 v114, 1088, v98
v_add_u32 v114, v114, v99
v_add_u32 v114, 147456, v114
v_bfe_u32 v1, v0, 2, 2
v_mul_u32_u24 v115, 1088, v1
v_lshrrev_b32 v1, 4, v0
v_lshl_add_u32 v115, v1, 5, v115
v_and_b32 v1, 3, v0
v_lshl_add_u32 v115, v1, 3, v115
v_add_u32 v115, 147456, v115
v_and_b32 v1, 1, v98
v_lshl_add_u32 v116, v1, 10, v99
v_lshlrev_b32 v117, 2, v98
s_lshr_b32 s51, s63, 1
s_lshl_b32 s51, s51, 11
s_and_b32 s52, s63, 1
s_lshl_b32 s52, s52, 9
s_add_u32 s51, s51, s52
s_lshl_b32 s52, s69, 18
s_add_u32 s51, s51, s52
s_add_u32 s51, s51, 1048576
s_add_u32 s52, s51, 1048576
s_lshl_b32 s53, s63, 7
s_add_u32 s67, s53, 131072
s_add_u32 s53, s53, 4096
v_mov_b32 v104, 0
s_waitcnt vmcnt(14)
v_lshlrev_b32 v82, 16, v64
v_and_b32 v83, 0xffff0000, v64
v_lshlrev_b32 v84, 16, v65
v_and_b32 v85, 0xffff0000, v65
v_lshlrev_b32 v86, 16, v66
v_and_b32 v87, 0xffff0000, v66
v_lshlrev_b32 v88, 16, v67
v_and_b32 v89, 0xffff0000, v67
s_waitcnt vmcnt(13)
v_lshlrev_b32 v90, 16, v68
v_and_b32 v91, 0xffff0000, v68
v_lshlrev_b32 v92, 16, v69
v_and_b32 v93, 0xffff0000, v69
v_lshlrev_b32 v94, 16, v70
v_and_b32 v95, 0xffff0000, v70
v_lshlrev_b32 v96, 16, v71
v_and_b32 v97, 0xffff0000, v71
v_pk_add_f32 v[100:101], v[82:83], v[84:85]
v_pk_add_f32 v[102:103], v[86:87], v[88:89]
v_pk_add_f32 v[100:101], v[100:101], v[90:91]
v_pk_add_f32 v[102:103], v[102:103], v[92:93]
v_pk_add_f32 v[100:101], v[100:101], v[94:95]
v_pk_add_f32 v[102:103], v[102:103], v[96:97]
v_pk_add_f32 v[100:101], v[100:101], v[102:103]
v_add_f32 v100, v100, v101
s_waitcnt vmcnt(12)
v_dot2c_f32_bf16 v104, v64, v72
v_dot2c_f32_bf16 v104, v65, v73
v_add_f32_dpp v100, v100, v100 row_ror:8 row_mask:0xf bank_mask:0xf
v_dot2c_f32_bf16 v104, v66, v74
v_dot2c_f32_bf16 v104, v67, v75
v_add_f32_dpp v100, v100, v100 row_ror:4 row_mask:0xf bank_mask:0xf
s_waitcnt vmcnt(11)
v_dot2c_f32_bf16 v104, v68, v76
v_dot2c_f32_bf16 v104, v69, v77
v_add_f32_dpp v100, v100, v100 row_ror:2 row_mask:0xf bank_mask:0xf
v_dot2c_f32_bf16 v104, v70, v78
v_dot2c_f32_bf16 v104, v71, v79
v_add_f32_dpp v100, v100, v100 row_ror:1 row_mask:0xf bank_mask:0xf
v_mov_b32 v101, v100
s_nop 1
v_permlane16_swap_b32 v100, v101
v_add_f32 v100, v100, v101
v_mul_f32 v100, 0xbb000000, v100
v_pk_add_f32 v[82:83], v[82:83], v[100:101] op_sel_hi:[1,0]
v_pk_add_f32 v[84:85], v[84:85], v[100:101] op_sel_hi:[1,0]
v_pk_add_f32 v[86:87], v[86:87], v[100:101] op_sel_hi:[1,0]
v_pk_add_f32 v[88:89], v[88:89], v[100:101] op_sel_hi:[1,0]
v_pk_add_f32 v[90:91], v[90:91], v[100:101] op_sel_hi:[1,0]
v_pk_add_f32 v[92:93], v[92:93], v[100:101] op_sel_hi:[1,0]
v_pk_add_f32 v[94:95], v[94:95], v[100:101] op_sel_hi:[1,0]
v_pk_add_f32 v[96:97], v[96:97], v[100:101] op_sel_hi:[1,0]
v_pk_mul_f32 v[102:103], v[82:83], v[82:83]
v_pk_mul_f32 v[100:101], v[84:85], v[84:85]
v_pk_fma_f32 v[102:103], v[86:87], v[86:87], v[102:103]
v_pk_fma_f32 v[100:101], v[88:89], v[88:89], v[100:101]
v_pk_fma_f32 v[102:103], v[90:91], v[90:91], v[102:103]
v_pk_fma_f32 v[100:101], v[92:93], v[92:93], v[100:101]
v_pk_fma_f32 v[102:103], v[94:95], v[94:95], v[102:103]
v_pk_fma_f32 v[100:101], v[96:97], v[96:97], v[100:101]
v_pk_add_f32 v[102:103], v[102:103], v[100:101]
v_add_f32 v102, v102, v103
s_nop 1
v_add_f32_dpp v102, v102, v102 row_ror:8 row_mask:0xf bank_mask:0xf
v_add_f32_dpp v104, v104, v104 row_ror:8 row_mask:0xf bank_mask:0xf
s_nop 0
v_add_f32_dpp v102, v102, v102 row_ror:4 row_mask:0xf bank_mask:0xf
v_add_f32_dpp v104, v104, v104 row_ror:4 row_mask:0xf bank_mask:0xf
s_nop 0
v_add_f32_dpp v102, v102, v102 row_ror:2 row_mask:0xf bank_mask:0xf
v_add_f32_dpp v104, v104, v104 row_ror:2 row_mask:0xf bank_mask:0xf
s_nop 0
v_add_f32_dpp v102, v102, v102 row_ror:1 row_mask:0xf bank_mask:0xf
v_add_f32_dpp v104, v104, v104 row_ror:1 row_mask:0xf bank_mask:0xf
v_mov_b32 v100, v102
v_mov_b32 v101, v104
s_nop 0
v_permlane16_swap_b32 v102, v100
v_permlane16_swap_b32 v104, v101
v_add_f32 v102, v102, v100
v_add_f32 v104, v104, v101
v_mov_b32 v100, 0x3727c5ac
v_fmac_f32 v100, 0x3b000000, v102
v_rsq_f32 v100, v100
v_mul_f32 v104, 0xbfb8aa3b, v104
v_pk_mul_f32 v[82:83], v[82:83], v[100:101] op_sel_hi:[1,0]
v_pk_mul_f32 v[84:85], v[84:85], v[100:101] op_sel_hi:[1,0]
v_pk_mul_f32 v[86:87], v[86:87], v[100:101] op_sel_hi:[1,0]
v_pk_mul_f32 v[88:89], v[88:89], v[100:101] op_sel_hi:[1,0]
v_pk_mul_f32 v[90:91], v[90:91], v[100:101] op_sel_hi:[1,0]
v_pk_mul_f32 v[92:93], v[92:93], v[100:101] op_sel_hi:[1,0]
v_pk_mul_f32 v[94:95], v[94:95], v[100:101] op_sel_hi:[1,0]
v_pk_mul_f32 v[96:97], v[96:97], v[100:101] op_sel_hi:[1,0]
v_cvt_pk_bf16_f32 v64, v82, v83
v_cvt_pk_bf16_f32 v65, v84, v85
v_cvt_pk_bf16_f32 v66, v86, v87
v_cvt_pk_bf16_f32 v67, v88, v89
ds_write_b128 v114, v[64:67]
v_cvt_pk_bf16_f32 v68, v90, v91
v_cvt_pk_bf16_f32 v69, v92, v93
v_cvt_pk_bf16_f32 v70, v94, v95
v_cvt_pk_bf16_f32 v71, v96, v97
ds_write_b128 v114, v[68:71] offset:512
v_exp_f32 v104, v104
s_nop 0
v_add_f32 v104, 1.0, v104
v_rcp_f32 v104, v104
s_waitcnt lgkmcnt(0)
s_barrier
ds_read_b64_tr_b16 v[106:107], v115 offset:0
ds_read_b64_tr_b16 v[108:109], v115 offset:4352
ds_read_b64_tr_b16 v[110:111], v115 offset:512
ds_read_b64_tr_b16 v[112:113], v115 offset:4864
s_mov_b32 exec_lo, 1
s_mov_b32 exec_hi, 1
buffer_store_dword v104, v117, s[12:15], s67 offen sc1
s_mov_b64 exec, -1
s_add_u32 s46, s46, 0x9e3779b9
s_add_u32 s47, s47, 0x7f4a7c15
s_waitcnt lgkmcnt(2)
buffer_store_dwordx4 v[106:109], v116, s[12:15], s51 offen sc1
s_waitcnt lgkmcnt(0)
buffer_store_dwordx4 v[110:113], v116, s[12:15], s52 offen sc1
s_add_u32 m0, s48, 12288
s_add_u32 s49, s49, 25088
s_cmp_eq_u32 s49, s65
s_cselect_b32 s49, s64, s49
buffer_load_dwordx4 v244, s[16:19], s49 offen offset:0 lds
s_add_u32 s49, s49, 20992
buffer_load_dwordx4 v244, s[16:19], s49 offen offset:1024 lds
s_add_u32 s49, s49, 20992
buffer_load_dwordx4 v244, s[16:19], s49 offen offset:2048 lds
s_add_u32 s49, s49, 20992
buffer_load_dwordx4 v244, s[16:19], s49 offen offset:3072 lds
s_add_u32 m0, s48, 16384
s_add_u32 s49, s49, 25088
s_cmp_eq_u32 s49, s65
s_cselect_b32 s49, s64, s49
buffer_load_dwordx4 v244, s[16:19], s49 offen offset:0 lds
s_add_u32 s49, s49, 20992
buffer_load_dwordx4 v244, s[16:19], s49 offen offset:1024 lds
s_add_u32 s49, s49, 20992
buffer_load_dwordx4 v244, s[16:19], s49 offen offset:2048 lds
s_add_u32 s49, s49, 20992
buffer_load_dwordx4 v244, s[16:19], s49 offen offset:3072 lds
s_add_u32 m0, s48, 20480
s_add_u32 s49, s49, 25088
s_cmp_eq_u32 s49, s65
s_cselect_b32 s49, s64, s49
buffer_load_dwordx4 v244, s[16:19], s49 offen offset:0 lds
s_add_u32 s49, s49, 20992
buffer_load_dwordx4 v244, s[16:19], s49 offen offset:1024 lds
s_add_u32 s49, s49, 20992
buffer_load_dwordx4 v244, s[16:19], s49 offen offset:2048 lds
s_add_u32 s49, s49, 20992
buffer_load_dwordx4 v244, s[16:19], s49 offen offset:3072 lds
s_waitcnt vmcnt(12)
s_barrier
s_cmp_lg_u32 s69, 0
s_cbranch_scc1 FL_14
v_mov_b32 v250, s46
v_mov_b32 v251, s47
s_mov_b64 exec, 1
buffer_store_dwordx2 v[250:251], off, s[12:15], s53 sc1
s_mov_b64 exec, -1
FL_14:
s_lshr_b32 s51, s69, 1
s_lshl_b32 s51, s51, 6
s_lshl_b32 s52, s55, 7
s_add_u32 s51, s51, s52
v_lshrrev_b32 v1, 5, v240
v_lshl_add_u32 v1, v1, 3, s51
buffer_load_dwordx2 v[16:17], v1, s[32:35], 0 offen offset:0
buffer_load_dwordx2 v[18:19], v1, s[32:35], 0 offen offset:16
buffer_load_dwordx2 v[20:21], v1, s[32:35], 0 offen offset:32
buffer_load_dwordx2 v[22:23], v1, s[32:35], 0 offen offset:48
s_lshl_b32 s51, s69, 6
s_add_u32 s51, s51, 32
v_add_lshl_u32 v249, v240, s51, 7
s_lshl_b32 s51, s56, 18
s_lshl_b32 s52, s69, 16
s_add_u32 s51, s51, s52
s_add_u32 s59, s51, 1048576
s_mov_b32 s66, s59
s_add_u32 s68, s59, 65536
s_bfe_u32 s51, s45, 0x40000
s_mul_i32 s51, s51, 4096
s_add_u32 s59, s59, s51
s_bfe_u32 s51, s69, 0x10000
s_lshl_b32 s51, s51, 10
v_add_u32 v245, s51, v241
v_xor_b32 v246, 0x400, v245
s_bfe_u32 s54, s69, 0x10001
s_lshl_b32 s54, s54, 6
v_lshrrev_b32 v248, 5, v240
v_lshl_add_u32 v248, v248, 10, s48
v_xad_u32 v247, v242, s54, v248
v_xor_b32 v248, 64, v247
ds_read_b64_tr_b16 a[0:1], v247 offset:0
ds_read_b64_tr_b16 a[2:3], v247 offset:512
ds_read_b64_tr_b16 a[4:5], v248 offset:0
ds_read_b64_tr_b16 a[6:7], v248 offset:512
ds_read_b64_tr_b16 a[8:9], v247 offset:2048
ds_read_b64_tr_b16 a[10:11], v247 offset:2560
ds_read_b64_tr_b16 a[12:13], v248 offset:2048
ds_read_b64_tr_b16 a[14:15], v248 offset:2560
s_waitcnt lgkmcnt(4)
s_add_u32 m0, s48, 0
s_add_u32 s49, s49, 25088
s_cmp_eq_u32 s49, s65
s_cselect_b32 s49, s64, s49
buffer_load_dwordx4 v244, s[16:19], s49 offen offset:0 lds
s_add_u32 s49, s49, 20992
buffer_load_dwordx4 v244, s[16:19], s49 offen offset:1024 lds
s_waitcnt lgkmcnt(0)
s_add_u32 s49, s49, 20992
buffer_load_dwordx4 v244, s[16:19], s49 offen offset:2048 lds
s_add_u32 s49, s49, 20992
buffer_load_dwordx4 v244, s[16:19], s49 offen offset:3072 lds
ds_read_b64_tr_b16 a[16:17], v247 offset:4096
ds_read_b64_tr_b16 a[18:19], v247 offset:4608
ds_read_b64_tr_b16 a[20:21], v248 offset:4096
ds_read_b64_tr_b16 a[22:23], v248 offset:4608
ds_read_b64_tr_b16 a[24:25], v247 offset:6144
ds_read_b64_tr_b16 a[26:27], v247 offset:6656
ds_read_b64_tr_b16 a[28:29], v248 offset:6144
ds_read_b64_tr_b16 a[30:31], v248 offset:6656
s_waitcnt lgkmcnt(4)
s_add_u32 m0, s48, 4096
s_add_u32 s49, s49, 25088
s_cmp_eq_u32 s49, s65
s_cselect_b32 s49, s64, s49
buffer_load_dwordx4 v244, s[16:19], s49 offen offset:0 lds
s_add_u32 s49, s49, 20992
buffer_load_dwordx4 v244, s[16:19], s49 offen offset:1024 lds
s_waitcnt lgkmcnt(0)
s_add_u32 s49, s49, 20992
buffer_load_dwordx4 v244, s[16:19], s49 offen offset:2048 lds
s_add_u32 s49, s49, 20992
buffer_load_dwordx4 v244, s[16:19], s49 offen offset:3072 lds
ds_read_b64_tr_b16 a[32:33], v247 offset:8192
ds_read_b64_tr_b16 a[34:35], v247 offset:8704
ds_read_b64_tr_b16 a[36:37], v248 offset:8192
ds_read_b64_tr_b16 a[38:39], v248 offset:8704
ds_read_b64_tr_b16 a[40:41], v247 offset:10240
ds_read_b64_tr_b16 a[42:43], v247 offset:10752
ds_read_b64_tr_b16 a[44:45], v248 offset:10240
ds_read_b64_tr_b16 a[46:47], v248 offset:10752
s_waitcnt lgkmcnt(4)
s_add_u32 m0, s48, 8192
s_add_u32 s49, s49, 25088
s_cmp_eq_u32 s49, s65
s_cselect_b32 s49, s64, s49
buffer_load_dwordx4 v244, s[16:19], s49 offen offset:0 lds
s_add_u32 s49, s49, 20992
buffer_load_dwordx4 v244, s[16:19], s49 offen offset:1024 lds
s_waitcnt lgkmcnt(0)
s_add_u32 s49, s49, 20992
buffer_load_dwordx4 v244, s[16:19], s49 offen offset:2048 lds
s_add_u32 s49, s49, 20992
buffer_load_dwordx4 v244, s[16:19], s49 offen offset:3072 lds
s_waitcnt vmcnt(26)
ds_read_b64_tr_b16 a[48:49], v247 offset:12288
ds_read_b64_tr_b16 a[50:51], v247 offset:12800
ds_read_b64_tr_b16 a[52:53], v248 offset:12288
ds_read_b64_tr_b16 a[54:55], v248 offset:12800
s_waitcnt vmcnt(24)
ds_read_b64_tr_b16 a[56:57], v247 offset:14336
ds_read_b64_tr_b16 a[58:59], v247 offset:14848
ds_read_b64_tr_b16 a[60:61], v248 offset:14336
ds_read_b64_tr_b16 a[62:63], v248 offset:14848
s_waitcnt lgkmcnt(4)
s_add_u32 m0, s48, 12288
s_add_u32 s49, s49, 25088
s_cmp_eq_u32 s49, s65
s_cselect_b32 s49, s64, s49
buffer_load_dwordx4 v244, s[16:19], s49 offen offset:0 lds
s_add_u32 s49, s49, 20992
buffer_load_dwordx4 v244, s[16:19], s49 offen offset:1024 lds
s_waitcnt lgkmcnt(0)
s_add_u32 s49, s49, 20992
buffer_load_dwordx4 v244, s[16:19], s49 offen offset:2048 lds
s_add_u32 s49, s49, 20992
buffer_load_dwordx4 v244, s[16:19], s49 offen offset:3072 lds
s_waitcnt vmcnt(26)
ds_read_b64_tr_b16 a[64:65], v247 offset:16384
ds_read_b64_tr_b16 a[66:67], v247 offset:16896
ds_read_b64_tr_b16 a[68:69], v248 offset:16384
ds_read_b64_tr_b16 a[70:71], v248 offset:16896
s_waitcnt vmcnt(24)
ds_read_b64_tr_b16 a[72:73], v247 offset:18432
ds_read_b64_tr_b16 a[74:75], v247 offset:18944
ds_read_b64_tr_b16 a[76:77], v248 offset:18432
ds_read_b64_tr_b16 a[78:79], v248 offset:18944
s_waitcnt lgkmcnt(4)
s_add_u32 m0, s48, 16384
s_add_u32 s49, s49, 25088
s_cmp_eq_u32 s49, s65
s_cselect_b32 s49, s64, s49
buffer_load_dwordx4 v244, s[16:19], s49 offen offset:0 lds
s_add_u32 s49, s49, 20992
buffer_load_dwordx4 v244, s[16:19], s49 offen offset:1024 lds
s_waitcnt lgkmcnt(0)
s_add_u32 s49, s49, 20992
buffer_load_dwordx4 v244, s[16:19], s49 offen offset:2048 lds
s_add_u32 s49, s49, 20992
buffer_load_dwordx4 v244, s[16:19], s49 offen offset:3072 lds
s_waitcnt vmcnt(26)
ds_read_b64_tr_b16 a[80:81], v247 offset:20480
ds_read_b64_tr_b16 a[82:83], v247 offset:20992
ds_read_b64_tr_b16 a[84:85], v248 offset:20480
ds_read_b64_tr_b16 a[86:87], v248 offset:20992
s_waitcnt vmcnt(24)
ds_read_b64_tr_b16 a[88:89], v247 offset:22528
ds_read_b64_tr_b16 a[90:91], v247 offset:23040
ds_read_b64_tr_b16 a[92:93], v248 offset:22528
ds_read_b64_tr_b16 a[94:95], v248 offset:23040
s_waitcnt lgkmcnt(4)
s_add_u32 m0, s48, 20480
s_add_u32 s49, s49, 25088
s_cmp_eq_u32 s49, s65
s_cselect_b32 s49, s64, s49
buffer_load_dwordx4 v244, s[16:19], s49 offen offset:0 lds
s_add_u32 s49, s49, 20992
buffer_load_dwordx4 v244, s[16:19], s49 offen offset:1024 lds
s_waitcnt lgkmcnt(0)
s_add_u32 s49, s49, 20992
buffer_load_dwordx4 v244, s[16:19], s49 offen offset:2048 lds
s_add_u32 s49, s49, 20992
buffer_load_dwordx4 v244, s[16:19], s49 offen offset:3072 lds
s_waitcnt vmcnt(22)
ds_read_b64_tr_b16 a[96:97], v247 offset:0
ds_read_b64_tr_b16 a[98:99], v247 offset:512
ds_read_b64_tr_b16 a[100:101], v248 offset:0
ds_read_b64_tr_b16 a[102:103], v248 offset:512
s_waitcnt vmcnt(20)
ds_read_b64_tr_b16 a[104:105], v247 offset:2048
ds_read_b64_tr_b16 a[106:107], v247 offset:2560
ds_read_b64_tr_b16 a[108:109], v248 offset:2048
ds_read_b64_tr_b16 a[110:111], v248 offset:2560
s_waitcnt lgkmcnt(4)
s_add_u32 m0, s48, 0
s_add_u32 s49, s49, 25088
s_cmp_eq_u32 s49, s65
s_cselect_b32 s49, s64, s49
buffer_load_dwordx4 v244, s[16:19], s49 offen offset:0 lds
s_add_u32 s49, s49, 20992
buffer_load_dwordx4 v244, s[16:19], s49 offen offset:1024 lds
s_waitcnt lgkmcnt(0)
s_add_u32 s49, s49, 20992
buffer_load_dwordx4 v244, s[16:19], s49 offen offset:2048 lds
s_add_u32 s49, s49, 20992
buffer_load_dwordx4 v244, s[16:19], s49 offen offset:3072 lds
s_waitcnt vmcnt(22)
ds_read_b64_tr_b16 a[112:113], v247 offset:4096
ds_read_b64_tr_b16 a[114:115], v247 offset:4608
ds_read_b64_tr_b16 a[116:117], v248 offset:4096
ds_read_b64_tr_b16 a[118:119], v248 offset:4608
s_waitcnt vmcnt(20)
ds_read_b64_tr_b16 a[120:121], v247 offset:6144
ds_read_b64_tr_b16 a[122:123], v247 offset:6656
ds_read_b64_tr_b16 a[124:125], v248 offset:6144
ds_read_b64_tr_b16 a[126:127], v248 offset:6656
s_waitcnt lgkmcnt(4)
s_add_u32 m0, s48, 4096
s_add_u32 s49, s49, 25088
s_cmp_eq_u32 s49, s65
s_cselect_b32 s49, s64, s49
buffer_load_dwordx4 v244, s[16:19], s49 offen offset:0 lds
s_add_u32 s49, s49, 20992
buffer_load_dwordx4 v244, s[16:19], s49 offen offset:1024 lds
s_waitcnt lgkmcnt(0)
s_add_u32 s49, s49, 20992
buffer_load_dwordx4 v244, s[16:19], s49 offen offset:2048 lds
s_add_u32 s49, s49, 20992
buffer_load_dwordx4 v244, s[16:19], s49 offen offset:3072 lds
buffer_load_dwordx2 v[250:251], v249, s[12:15], 0 offen sc1
s_waitcnt vmcnt(23)
ds_read_b64_tr_b16 a[128:129], v247 offset:8192
ds_read_b64_tr_b16 a[130:131], v247 offset:8704
ds_read_b64_tr_b16 a[132:133], v248 offset:8192
ds_read_b64_tr_b16 a[134:135], v248 offset:8704
s_waitcnt vmcnt(21)
ds_read_b64_tr_b16 a[136:137], v247 offset:10240
ds_read_b64_tr_b16 a[138:139], v247 offset:10752
ds_read_b64_tr_b16 a[140:141], v248 offset:10240
ds_read_b64_tr_b16 a[142:143], v248 offset:10752
s_waitcnt lgkmcnt(4)
s_add_u32 m0, s48, 8192
s_add_u32 s49, s49, 25088
s_cmp_eq_u32 s49, s65
s_cselect_b32 s49, s64, s49
buffer_load_dwordx4 v244, s[16:19], s49 offen offset:0 lds
s_add_u32 s49, s49, 20992
buffer_load_dwordx4 v244, s[16:19], s49 offen offset:1024 lds
s_waitcnt lgkmcnt(0)
s_add_u32 s49, s49, 20992
buffer_load_dwordx4 v244, s[16:19], s49 offen offset:2048 lds
s_add_u32 s49, s49, 20992
buffer_load_dwordx4 v244, s[16:19], s49 offen offset:3072 lds
s_waitcnt vmcnt(23)
ds_read_b64_tr_b16 a[144:145], v247 offset:12288
ds_read_b64_tr_b16 a[146:147], v247 offset:12800
ds_read_b64_tr_b16 a[148:149], v248 offset:12288
ds_read_b64_tr_b16 a[150:151], v248 offset:12800
s_waitcnt vmcnt(21)
ds_read_b64_tr_b16 a[152:153], v247 offset:14336
ds_read_b64_tr_b16 a[154:155], v247 offset:14848
ds_read_b64_tr_b16 a[156:157], v248 offset:14336
ds_read_b64_tr_b16 a[158:159], v248 offset:14848
s_waitcnt lgkmcnt(4)
s_add_u32 m0, s48, 12288
s_add_u32 s49, s49, 25088
s_cmp_eq_u32 s49, s65
s_cselect_b32 s49, s64, s49
buffer_load_dwordx4 v244, s[16:19], s49 offen offset:0 lds
s_add_u32 s49, s49, 20992
buffer_load_dwordx4 v244, s[16:19], s49 offen offset:1024 lds
s_waitcnt lgkmcnt(0)
s_add_u32 s49, s49, 20992
buffer_load_dwordx4 v244, s[16:19], s49 offen offset:2048 lds
s_waitcnt vmcnt(22)
ds_read_b64_tr_b16 a[160:161], v247 offset:16384
ds_read_b64_tr_b16 a[162:163], v247 offset:16896
ds_read_b64_tr_b16 a[164:165], v248 offset:16384
ds_read_b64_tr_b16 a[166:167], v248 offset:16896
s_waitcnt vmcnt(20)
ds_read_b64_tr_b16 a[168:169], v247 offset:18432
ds_read_b64_tr_b16 a[170:171], v247 offset:18944
ds_read_b64_tr_b16 a[172:173], v248 offset:18432
ds_read_b64_tr_b16 a[174:175], v248 offset:18944
s_waitcnt vmcnt(18)
ds_read_b64_tr_b16 a[176:177], v247 offset:20480
ds_read_b64_tr_b16 a[178:179], v247 offset:20992
ds_read_b64_tr_b16 a[180:181], v248 offset:20480
ds_read_b64_tr_b16 a[182:183], v248 offset:20992
s_waitcnt vmcnt(16)
ds_read_b64_tr_b16 a[184:185], v247 offset:22528
ds_read_b64_tr_b16 a[186:187], v247 offset:23040
ds_read_b64_tr_b16 a[188:189], v248 offset:22528
ds_read_b64_tr_b16 a[190:191], v248 offset:23040
s_waitcnt vmcnt(14)
ds_read_b64_tr_b16 a[192:193], v247 offset:0
ds_read_b64_tr_b16 a[194:195], v247 offset:512
ds_read_b64_tr_b16 a[196:197], v248 offset:0
ds_read_b64_tr_b16 a[198:199], v248 offset:512
s_waitcnt vmcnt(12)
ds_read_b64_tr_b16 a[200:201], v247 offset:2048
ds_read_b64_tr_b16 a[202:203], v247 offset:2560
ds_read_b64_tr_b16 a[204:205], v248 offset:2048
ds_read_b64_tr_b16 a[206:207], v248 offset:2560
s_waitcnt vmcnt(10)
ds_read_b64_tr_b16 a[208:209], v247 offset:4096
ds_read_b64_tr_b16 a[210:211], v247 offset:4608
ds_read_b64_tr_b16 a[212:213], v248 offset:4096
ds_read_b64_tr_b16 a[214:215], v248 offset:4608
s_waitcnt vmcnt(8)
ds_read_b64_tr_b16 a[216:217], v247 offset:6144
ds_read_b64_tr_b16 a[218:219], v247 offset:6656
ds_read_b64_tr_b16 a[220:221], v248 offset:6144
ds_read_b64_tr_b16 a[222:223], v248 offset:6656
v_lshlrev_b32 v0, 16, v16
v_and_b32 v1, 0xffff0000, v16
v_lshlrev_b32 v2, 16, v17
v_and_b32 v3, 0xffff0000, v17
v_lshlrev_b32 v4, 16, v18
v_and_b32 v5, 0xffff0000, v18
v_lshlrev_b32 v6, 16, v19
v_and_b32 v7, 0xffff0000, v19
v_lshlrev_b32 v8, 16, v20
v_and_b32 v9, 0xffff0000, v20
v_lshlrev_b32 v10, 16, v21
v_and_b32 v11, 0xffff0000, v21
v_lshlrev_b32 v12, 16, v22
v_and_b32 v13, 0xffff0000, v22
v_lshlrev_b32 v14, 16, v23
v_and_b32 v15, 0xffff0000, v23
s_waitcnt vmcnt(7)
v_cmp_ne_u64 vcc, s[46:47], v[250:251]
s_mov_b32 s54, 0
s_nop 0
s_cbranch_vccz POLLD_16
POLL_15:
buffer_load_dwordx2 v[250:251], v249, s[12:15], 0 offen sc1
s_waitcnt vmcnt(0)
v_cmp_ne_u64 vcc, s[46:47], v[250:251]
s_nop 1
s_cbranch_vccz POLLD_16
s_add_u32 s54, s54, 1
s_cmp_lt_u32 s54, 200000
s_sleep 1
s_cbranch_scc1 POLL_15
POLLD_16:
buffer_load_dwordx4 v[144:147], v245, s[12:15], s59 offen offset:0 sc1
buffer_load_dwordx4 v[148:151], v246, s[12:15], s59 offen offset:0 sc1
buffer_load_dwordx4 v[152:155], v245, s[12:15], s59 offen offset:2048 sc1
buffer_load_dwordx4 v[156:159], v246, s[12:15], s59 offen offset:2048 sc1
s_add_u32 s59, s59, 4096
s_cmp_eq_u32 s59, s68
s_cselect_b32 s59, s66, s59
buffer_load_dwordx4 v[160:163], v245, s[12:15], s59 offen offset:0 sc1
buffer_load_dwordx4 v[164:167], v246, s[12:15], s59 offen offset:0 sc1
buffer_load_dwordx4 v[168:171], v245, s[12:15], s59 offen offset:2048 sc1
buffer_load_dwordx4 v[172:175], v246, s[12:15], s59 offen offset:2048 sc1
s_add_u32 s59, s59, 4096
s_cmp_eq_u32 s59, s68
s_cselect_b32 s59, s66, s59
buffer_load_dwordx4 v[176:179], v245, s[12:15], s59 offen offset:0 sc1
buffer_load_dwordx4 v[180:183], v246, s[12:15], s59 offen offset:0 sc1
s_xor_b32 s51, s69, 1
s_mul_i32 s51, s51, 12288
s_add_u32 s51, s51, 98304
v_add_u32 v137, s51, v241
s_xor_b32 s51, s69, 2
s_mul_i32 s51, s51, 12288
s_add_u32 s51, s51, 102400
v_add_u32 v138, s51, v241
s_xor_b32 s51, s69, 3
s_mul_i32 s51, s51, 12288
s_add_u32 s51, s51, 106496
v_add_u32 v139, s51, v241
s_mul_i32 s51, s69, 12288
s_add_u32 s51, s51, 98304
v_add_u32 v136, s51, v241
s_waitcnt vmcnt(9)
v_mfma_f32_32x32x16_bf16 v[32:47], a[4:7], v[144:147], 0
s_add_u32 s49, s49, 20992
buffer_load_dwordx4 v244, s[16:19], s49 offen offset:3072 lds
ds_read_b64_tr_b16 a[224:225], v247 offset:8192
v_mfma_f32_32x32x16_bf16 v[0:15], a[0:3], v[144:147], v[0:15]
buffer_load_dwordx4 v[184:187], v245, s[12:15], s59 offen offset:2048 sc1
s_waitcnt vmcnt(10)
v_mfma_f32_32x32x16_bf16 v[16:31], a[0:3], v[148:151], 0
ds_read_b64_tr_b16 a[226:227], v247 offset:8704
ds_read_b64_tr_b16 a[228:229], v248 offset:8192
v_mfma_f32_32x32x16_bf16 v[48:63], a[4:7], v[148:151], 0
buffer_load_dwordx4 v[188:191], v246, s[12:15], s59 offen offset:2048 sc1
s_waitcnt vmcnt(10)
v_mfma_f32_32x32x16_bf16 v[32:47], a[12:15], v[152:155], v[32:47]
ds_read_b64_tr_b16 a[230:231], v248 offset:8704
ds_read_b64_tr_b16 a[232:233], v247 offset:10240
v_mfma_f32_32x32x16_bf16 v[0:15], a[8:11], v[152:155], v[0:15]
s_add_u32 s59, s59, 4096
s_cmp_eq_u32 s59, s68
s_cselect_b32 s59, s66, s59
buffer_load_dwordx4 v[192:195], v245, s[12:15], s59 offen offset:0 sc1
s_waitcnt vmcnt(10)
v_mfma_f32_32x32x16_bf16 v[16:31], a[8:11], v[156:159], v[16:31]
ds_read_b64_tr_b16 a[234:235], v247 offset:10752
ds_read_b64_tr_b16 a[236:237], v248 offset:10240
v_mfma_f32_32x32x16_bf16 v[48:63], a[12:15], v[156:159], v[48:63]
buffer_load_dwordx4 v[196:199], v246, s[12:15], s59 offen offset:0 sc1
s_waitcnt vmcnt(10)
v_mfma_f32_32x32x16_bf16 v[32:47], a[20:23], v[160:163], v[32:47]
ds_read_b64_tr_b16 a[238:239], v248 offset:10752
ds_read_b64_tr_b16 a[240:241], v247 offset:12288
v_mfma_f32_32x32x16_bf16 v[0:15], a[16:19], v[160:163], v[0:15]
buffer_load_dwordx4 v[144:147], v245, s[12:15], s59 offen offset:2048 sc1
s_waitcnt vmcnt(10)
v_mfma_f32_32x32x16_bf16 v[16:31], a[16:19], v[164:167], v[16:31]
ds_read_b64_tr_b16 a[242:243], v247 offset:12800
ds_read_b64_tr_b16 a[244:245], v248 offset:12288
v_mfma_f32_32x32x16_bf16 v[48:63], a[20:23], v[164:167], v[48:63]
buffer_load_dwordx4 v[148:151], v246, s[12:15], s59 offen offset:2048 sc1
s_waitcnt vmcnt(10)
v_mfma_f32_32x32x16_bf16 v[32:47], a[28:31], v[168:171], v[32:47]
ds_read_b64_tr_b16 a[246:247], v248 offset:12800
v_mfma_f32_32x32x16_bf16 v[0:15], a[24:27], v[168:171], v[0:15]
s_add_u32 s59, s59, 4096
s_cmp_eq_u32 s59, s68
s_cselect_b32 s59, s66, s59
buffer_load_dwordx4 v[152:155], v245, s[12:15], s59 offen offset:0 sc1
s_waitcnt vmcnt(10)
v_mfma_f32_32x32x16_bf16 v[16:31], a[24:27], v[172:175], v[16:31]
v_mfma_f32_32x32x16_bf16 v[48:63], a[28:31], v[172:175], v[48:63]
buffer_load_dwordx4 v[156:159], v246, s[12:15], s59 offen offset:0 sc1
s_waitcnt vmcnt(10)
v_mfma_f32_32x32x16_bf16 v[32:47], a[36:39], v[176:179], v[32:47]
v_mfma_f32_32x32x16_bf16 v[0:15], a[32:35], v[176:179], v[0:15]
buffer_load_dwordx4 v[160:163], v245, s[12:15], s59 offen offset:2048 sc1
s_waitcnt vmcnt(10)
v_mfma_f32_32x32x16_bf16 v[16:31], a[32:35], v[180:183], v[16:31]
v_mfma_f32_32x32x16_bf16 v[48:63], a[36:39], v[180:183], v[48:63]
buffer_load_dwordx4 v[164:167], v246, s[12:15], s59 offen offset:2048 sc1
s_waitcnt vmcnt(9)
v_mfma_f32_32x32x16_bf16 v[32:47], a[44:47], v[184:187], v[32:47]
ds_read_b64_tr_b16 a[248:249], v247 offset:14336
ds_read_b64_tr_b16 a[250:251], v247 offset:14848
v_mfma_f32_32x32x16_bf16 v[0:15], a[40:43], v[184:187], v[0:15]
s_add_u32 s59, s59, 4096
s_cmp_eq_u32 s59, s68
s_cselect_b32 s59, s66, s59
buffer_load_dwordx4 v[168:171], v245, s[12:15], s59 offen offset:0 sc1
s_waitcnt vmcnt(9)
v_mfma_f32_32x32x16_bf16 v[16:31], a[40:43], v[188:191], v[16:31]
ds_read_b64_tr_b16 a[252:253], v248 offset:14336
ds_read_b64_tr_b16 a[254:255], v248 offset:14848
v_mfma_f32_32x32x16_bf16 v[48:63], a[44:47], v[188:191], v[48:63]
buffer_load_dwordx4 v[172:175], v246, s[12:15], s59 offen offset:0 sc1
s_waitcnt vmcnt(9)
v_mfma_f32_32x32x16_bf16 v[32:47], a[52:55], v[192:195], v[32:47]
s_mul_i32 s49, s69, 1441792
s_lshl_b32 s51, s57, 7
s_add_u32 s49, s49, s51
s_lshl_b32 s50, s69, 17
v_mfma_f32_32x32x16_bf16 v[0:15], a[48:51], v[192:195], v[0:15]
buffer_load_dwordx4 v[176:179], v245, s[12:15], s59 offen offset:2048 sc1
s_waitcnt vmcnt(9)
v_mfma_f32_32x32x16_bf16 v[16:31], a[48:51], v[196:199], v[16:31]
s_lshl_b32 s51, s58, 7
s_add_u32 s50, s50, s51
s_mov_b32 s64, s49
s_add_u32 s65, s49, 1441792
v_mfma_f32_32x32x16_bf16 v[48:63], a[52:55], v[196:199], v[48:63]
buffer_load_dwordx4 v[180:183], v246, s[12:15], s59 offen offset:2048 sc1
s_waitcnt vmcnt(9)
v_mfma_f32_32x32x16_bf16 v[32:47], a[60:63], v[144:147], v[32:47]
s_bfe_u32 s51, s45, 0x40004
s_mul_i32 s51, s51, 131072
s_add_u32 s49, s49, s51
s_mov_b32 s0, s50
v_mfma_f32_32x32x16_bf16 v[0:15], a[56:59], v[144:147], v[0:15]
s_add_u32 s59, s59, 4096
s_cmp_eq_u32 s59, s68
s_cselect_b32 s59, s66, s59
buffer_load_dwordx4 v[184:187], v245, s[12:15], s59 offen offset:0 sc1
s_waitcnt vmcnt(9)
v_mfma_f32_32x32x16_bf16 v[16:31], a[56:59], v[148:151], v[16:31]
s_add_u32 s1, s50, 131072
s_bfe_u32 s51, s45, 0x20008
s_mul_i32 s51, s51, 32768
s_add_u32 s50, s50, s51
v_mfma_f32_32x32x16_bf16 v[48:63], a[60:63], v[148:151], v[48:63]
buffer_load_dwordx4 v[188:191], v246, s[12:15], s59 offen offset:0 sc1
s_waitcnt vmcnt(9)
v_mfma_f32_32x32x16_bf16 v[32:47], a[68:71], v[152:155], v[32:47]
v_mfma_f32_32x32x16_bf16 v[0:15], a[64:67], v[152:155], v[0:15]
buffer_load_dwordx4 v[192:195], v245, s[12:15], s59 offen offset:2048 sc1
s_waitcnt vmcnt(9)
v_mfma_f32_32x32x16_bf16 v[16:31], a[64:67], v[156:159], v[16:31]
v_mfma_f32_32x32x16_bf16 v[48:63], a[68:71], v[156:159], v[48:63]
buffer_load_dwordx4 v[196:199], v246, s[12:15], s59 offen offset:2048 sc1
s_waitcnt vmcnt(9)
v_mfma_f32_32x32x16_bf16 v[32:47], a[76:79], v[160:163], v[32:47]
v_mfma_f32_32x32x16_bf16 v[0:15], a[72:75], v[160:163], v[0:15]
s_add_u32 s59, s59, 4096
s_cmp_eq_u32 s59, s68
s_cselect_b32 s59, s66, s59
buffer_load_dwordx4 v[144:147], v245, s[12:15], s59 offen offset:0 sc1
s_waitcnt vmcnt(9)
v_mfma_f32_32x32x16_bf16 v[16:31], a[72:75], v[164:167], v[16:31]
v_mfma_f32_32x32x16_bf16 v[48:63], a[76:79], v[164:167], v[48:63]
buffer_load_dwordx4 v[148:151], v246, s[12:15], s59 offen offset:0 sc1
s_waitcnt vmcnt(9)
v_mfma_f32_32x32x16_bf16 v[32:47], a[84:87], v[168:171], v[32:47]
v_mfma_f32_32x32x16_bf16 v[0:15], a[80:83], v[168:171], v[0:15]
buffer_load_dwordx4 v[152:155], v245, s[12:15], s59 offen offset:2048 sc1
s_waitcnt vmcnt(9)
v_mfma_f32_32x32x16_bf16 v[16:31], a[80:83], v[172:175], v[16:31]
v_mfma_f32_32x32x16_bf16 v[48:63], a[84:87], v[172:175], v[48:63]
buffer_load_dwordx4 v[156:159], v246, s[12:15], s59 offen offset:2048 sc1
s_waitcnt vmcnt(9)
v_mfma_f32_32x32x16_bf16 v[32:47], a[92:95], v[176:179], v[32:47]
v_mfma_f32_32x32x16_bf16 v[0:15], a[88:91], v[176:179], v[0:15]
s_add_u32 s59, s59, 4096
s_cmp_eq_u32 s59, s68
s_cselect_b32 s59, s66, s59
buffer_load_dwordx4 v[160:163], v245, s[12:15], s59 offen offset:0 sc1
s_waitcnt vmcnt(9)
v_mfma_f32_32x32x16_bf16 v[16:31], a[88:91], v[180:183], v[16:31]
v_mfma_f32_32x32x16_bf16 v[48:63], a[92:95], v[180:183], v[48:63]
buffer_load_dwordx4 v[164:167], v246, s[12:15], s59 offen offset:0 sc1
s_waitcnt vmcnt(9)
v_mfma_f32_32x32x16_bf16 v[32:47], a[100:103], v[184:187], v[32:47]
v_mfma_f32_32x32x16_bf16 v[0:15], a[96:99], v[184:187], v[0:15]
buffer_load_dwordx4 v[168:171], v245, s[12:15], s59 offen offset:2048 sc1
s_waitcnt vmcnt(9)
v_mfma_f32_32x32x16_bf16 v[16:31], a[96:99], v[188:191], v[16:31]
v_mfma_f32_32x32x16_bf16 v[48:63], a[100:103], v[188:191], v[48:63]
buffer_load_dwordx4 v[172:175], v246, s[12:15], s59 offen offset:2048 sc1
s_waitcnt vmcnt(9)
v_mfma_f32_32x32x16_bf16 v[32:47], a[108:111], v[192:195], v[32:47]
v_mfma_f32_32x32x16_bf16 v[0:15], a[104:107], v[192:195], v[0:15]
s_add_u32 s59, s59, 4096
s_cmp_eq_u32 s59, s68
s_cselect_b32 s59, s66, s59
buffer_load_dwordx4 v[176:179], v245, s[12:15], s59 offen offset:0 sc1
s_waitcnt vmcnt(9)
v_mfma_f32_32x32x16_bf16 v[16:31], a[104:107], v[196:199], v[16:31]
v_mfma_f32_32x32x16_bf16 v[48:63], a[108:111], v[196:199], v[48:63]
buffer_load_dwordx4 v[180:183], v246, s[12:15], s59 offen offset:0 sc1
s_waitcnt vmcnt(9)
v_mfma_f32_32x32x16_bf16 v[32:47], a[116:119], v[144:147], v[32:47]
v_mfma_f32_32x32x16_bf16 v[0:15], a[112:115], v[144:147], v[0:15]
buffer_load_dwordx4 v[184:187], v245, s[12:15], s59 offen offset:2048 sc1
s_waitcnt vmcnt(9)
v_mfma_f32_32x32x16_bf16 v[16:31], a[112:115], v[148:151], v[16:31]
v_mfma_f32_32x32x16_bf16 v[48:63], a[116:119], v[148:151], v[48:63]
buffer_load_dwordx4 v[188:191], v246, s[12:15], s59 offen offset:2048 sc1
s_waitcnt vmcnt(9)
v_mfma_f32_32x32x16_bf16 v[32:47], a[124:127], v[152:155], v[32:47]
v_mfma_f32_32x32x16_bf16 v[0:15], a[120:123], v[152:155], v[0:15]
s_add_u32 s59, s59, 4096
s_cmp_eq_u32 s59, s68
s_cselect_b32 s59, s66, s59
buffer_load_dwordx4 v[192:195], v245, s[12:15], s59 offen offset:0 sc1
s_waitcnt vmcnt(9)
v_mfma_f32_32x32x16_bf16 v[16:31], a[120:123], v[156:159], v[16:31]
v_mfma_f32_32x32x16_bf16 v[48:63], a[124:127], v[156:159], v[48:63]
buffer_load_dwordx4 v[196:199], v246, s[12:15], s59 offen offset:0 sc1
s_waitcnt vmcnt(9)
v_mfma_f32_32x32x16_bf16 v[32:47], a[132:135], v[160:163], v[32:47]
v_mfma_f32_32x32x16_bf16 v[0:15], a[128:131], v[160:163], v[0:15]
buffer_load_dwordx4 v[144:147], v245, s[12:15], s59 offen offset:2048 sc1
s_waitcnt vmcnt(9)
v_mfma_f32_32x32x16_bf16 v[16:31], a[128:131], v[164:167], v[16:31]
v_mfma_f32_32x32x16_bf16 v[48:63], a[132:135], v[164:167], v[48:63]
buffer_load_dwordx4 v[148:151], v246, s[12:15], s59 offen offset:2048 sc1
s_waitcnt vmcnt(9)
v_mfma_f32_32x32x16_bf16 v[32:47], a[140:143], v[168:171], v[32:47]
v_mfma_f32_32x32x16_bf16 v[0:15], a[136:139], v[168:171], v[0:15]
s_add_u32 s59, s59, 4096
s_cmp_eq_u32 s59, s68
s_cselect_b32 s59, s66, s59
buffer_load_dwordx4 v[152:155], v245, s[12:15], s59 offen offset:0 sc1
s_waitcnt vmcnt(9)
v_mfma_f32_32x32x16_bf16 v[16:31], a[136:139], v[172:175], v[16:31]
v_mfma_f32_32x32x16_bf16 v[48:63], a[140:143], v[172:175], v[48:63]
buffer_load_dwordx4 v[156:159], v246, s[12:15], s59 offen offset:0 sc1
s_waitcnt vmcnt(9)
v_mfma_f32_32x32x16_bf16 v[32:47], a[148:151], v[176:179], v[32:47]
v_mfma_f32_32x32x16_bf16 v[0:15], a[144:147], v[176:179], v[0:15]
buffer_load_dwordx4 v[160:163], v245, s[12:15], s59 offen offset:2048 sc1
s_waitcnt vmcnt(9)
v_mfma_f32_32x32x16_bf16 v[16:31], a[144:147], v[180:183], v[16:31]
v_mfma_f32_32x32x16_bf16 v[48:63], a[148:151], v[180:183], v[48:63]
buffer_load_dwordx4 v[164:167], v246, s[12:15], s59 offen offset:2048 sc1
s_waitcnt vmcnt(9)
v_mfma_f32_32x32x16_bf16 v[32:47], a[156:159], v[184:187], v[32:47]
v_mfma_f32_32x32x16_bf16 v[0:15], a[152:155], v[184:187], v[0:15]
s_add_u32 s59, s59, 4096
s_cmp_eq_u32 s59, s68
s_cselect_b32 s59, s66, s59
buffer_load_dwordx4 v[168:171], v245, s[12:15], s59 offen offset:0 sc1
s_waitcnt vmcnt(9)
v_mfma_f32_32x32x16_bf16 v[16:31], a[152:155], v[188:191], v[16:31]
v_mfma_f32_32x32x16_bf16 v[48:63], a[156:159], v[188:191], v[48:63]
buffer_load_dwordx4 v[172:175], v246, s[12:15], s59 offen offset:0 sc1
s_waitcnt vmcnt(9) lgkmcnt(15)
v_mfma_f32_32x32x16_bf16 v[32:47], a[164:167], v[192:195], v[32:47]
v_mfma_f32_32x32x16_bf16 v[0:15], a[160:163], v[192:195], v[0:15]
buffer_load_dwordx4 v[176:179], v245, s[12:15], s59 offen offset:2048 sc1
s_waitcnt vmcnt(9)
v_mfma_f32_32x32x16_bf16 v[16:31], a[160:163], v[196:199], v[16:31]
v_mfma_f32_32x32x16_bf16 v[48:63], a[164:167], v[196:199], v[48:63]
buffer_load_dwordx4 v[180:183], v246, s[12:15], s59 offen offset:2048 sc1
s_waitcnt vmcnt(9)
v_mfma_f32_32x32x16_bf16 v[32:47], a[172:175], v[144:147], v[32:47]
v_mfma_f32_32x32x16_bf16 v[0:15], a[168:171], v[144:147], v[0:15]
s_add_u32 s59, s59, 4096
s_cmp_eq_u32 s59, s68
s_cselect_b32 s59, s66, s59
s_mov_b32 s53, s59
buffer_load_dwordx4 v[184:187], v246, s[12:15], s59 offen offset:0 sc1
s_waitcnt vmcnt(9)
v_mfma_f32_32x32x16_bf16 v[16:31], a[168:171], v[148:151], v[16:31]
v_mfma_f32_32x32x16_bf16 v[48:63], a[172:175], v[148:151], v[48:63]
buffer_load_dwordx4 v[188:191], v246, s[12:15], s59 offen offset:2048 sc1
s_waitcnt vmcnt(9)
v_mfma_f32_32x32x16_bf16 v[32:47], a[180:183], v[152:155], v[32:47]
v_mfma_f32_32x32x16_bf16 v[0:15], a[176:179], v[152:155], v[0:15]
s_add_u32 s59, s59, 4096
s_cmp_eq_u32 s59, s68
s_cselect_b32 s59, s66, s59
buffer_load_dwordx4 v[192:195], v246, s[12:15], s59 offen offset:0 sc1
s_waitcnt vmcnt(9)
v_mfma_f32_32x32x16_bf16 v[16:31], a[176:179], v[156:159], v[16:31]
v_mfma_f32_32x32x16_bf16 v[48:63], a[180:183], v[156:159], v[48:63]
buffer_load_dwordx4 v[196:199], v246, s[12:15], s59 offen offset:2048 sc1
s_waitcnt vmcnt(9)
v_mfma_f32_32x32x16_bf16 v[32:47], a[188:191], v[160:163], v[32:47]
v_mfma_f32_32x32x16_bf16 v[0:15], a[184:187], v[160:163], v[0:15]
s_add_u32 s59, s59, 4096
s_cmp_eq_u32 s59, s68
s_cselect_b32 s59, s66, s59
buffer_load_dwordx4 v[144:147], v246, s[12:15], s59 offen offset:0 sc1
s_waitcnt vmcnt(9)
v_mfma_f32_32x32x16_bf16 v[16:31], a[184:187], v[164:167], v[16:31]
v_mfma_f32_32x32x16_bf16 v[48:63], a[188:191], v[164:167], v[48:63]
buffer_load_dwordx4 v[148:151], v246, s[12:15], s59 offen offset:2048 sc1
s_waitcnt vmcnt(9)
v_mfma_f32_32x32x16_bf16 v[32:47], a[196:199], v[168:171], v[32:47]
v_mfma_f32_32x32x16_bf16 v[0:15], a[192:195], v[168:171], v[0:15]
s_mov_b32 s59, s53
buffer_load_dwordx4 v[152:155], v245, s[12:15], s59 offen offset:0 sc1
s_waitcnt vmcnt(9)
v_mfma_f32_32x32x16_bf16 v[16:31], a[192:195], v[172:175], v[16:31]
v_mfma_f32_32x32x16_bf16 v[48:63], a[196:199], v[172:175], v[48:63]
buffer_load_dwordx4 v[156:159], v245, s[12:15], s59 offen offset:2048 sc1
s_waitcnt vmcnt(9)
v_mfma_f32_32x32x16_bf16 v[32:47], a[204:207], v[176:179], v[32:47]
v_mfma_f32_32x32x16_bf16 v[0:15], a[200:203], v[176:179], v[0:15]
s_add_u32 s59, s59, 4096
s_cmp_eq_u32 s59, s68
s_cselect_b32 s59, s66, s59
buffer_load_dwordx4 v[160:163], v245, s[12:15], s59 offen offset:0 sc1
s_waitcnt vmcnt(9)
v_mfma_f32_32x32x16_bf16 v[16:31], a[200:203], v[180:183], v[16:31]
v_mfma_f32_32x32x16_bf16 v[48:63], a[204:207], v[180:183], v[48:63]
buffer_load_dwordx4 v[164:167], v245, s[12:15], s59 offen offset:2048 sc1
s_waitcnt vmcnt(9)
v_mfma_f32_32x32x16_bf16 v[48:63], a[212:215], v[184:187], v[48:63]
v_mfma_f32_32x32x16_bf16 v[16:31], a[208:211], v[184:187], v[16:31]
s_add_u32 s59, s59, 4096
s_cmp_eq_u32 s59, s68
s_cselect_b32 s59, s66, s59
buffer_load_dwordx4 v[168:171], v245, s[12:15], s59 offen offset:0 sc1
s_waitcnt vmcnt(9)
v_mfma_f32_32x32x16_bf16 v[16:31], a[216:219], v[188:191], v[16:31]
v_mfma_f32_32x32x16_bf16 v[48:63], a[220:223], v[188:191], v[48:63]
buffer_load_dwordx4 v[172:175], v245, s[12:15], s59 offen offset:2048 sc1
s_waitcnt lgkmcnt(0)
s_waitcnt vmcnt(9)
v_mfma_f32_32x32x16_bf16 v[48:63], a[228:231], v[192:195], v[48:63]
v_mfma_f32_32x32x16_bf16 v[16:31], a[224:227], v[192:195], v[16:31]
s_waitcnt vmcnt(8)
v_mfma_f32_32x32x16_bf16 v[16:31], a[232:235], v[196:199], v[16:31]
v_mfma_f32_32x32x16_bf16 v[48:63], a[236:239], v[196:199], v[48:63]
s_waitcnt vmcnt(7)
v_mfma_f32_32x32x16_bf16 v[48:63], a[244:247], v[144:147], v[48:63]
v_mfma_f32_32x32x16_bf16 v[16:31], a[240:243], v[144:147], v[16:31]
v_lshrrev_b32 v244, 3, v240
v_mul_u32_u24 v244, 4096, v244
v_add_u32 v244, v244, v243
s_add_u32 m0, s48, 0
s_nop 0
buffer_load_dwordx4 v244, s[24:27], s49 offen offset:0 lds
s_waitcnt vmcnt(7)
v_mfma_f32_32x32x16_bf16 v[16:31], a[248:251], v[148:151], v[16:31]
v_mfma_f32_32x32x16_bf16 v[48:63], a[252:255], v[148:151], v[48:63]
s_add_u32 s49, s49, 31744
buffer_load_dwordx4 v244, s[24:27], s49 offen offset:1024 lds
s_waitcnt vmcnt(7)
v_mfma_f32_32x32x16_bf16 v[32:47], a[212:215], v[152:155], v[32:47]
s_nop 6
ds_write_b128 v137, v[16:19] offset:0
v_mfma_f32_32x32x16_bf16 v[0:15], a[208:211], v[152:155], v[0:15]
s_add_u32 s49, s49, 31744
buffer_load_dwordx4 v244, s[24:27], s49 offen offset:2048 lds
ds_write_b128 v137, v[20:23] offset:1024
s_waitcnt vmcnt(7)
v_mfma_f32_32x32x16_bf16 v[32:47], a[220:223], v[156:159], v[32:47]
ds_write_b128 v137, v[24:27] offset:2048
v_mfma_f32_32x32x16_bf16 v[0:15], a[216:219], v[156:159], v[0:15]
s_add_u32 s49, s49, 31744
buffer_load_dwordx4 v244, s[24:27], s49 offen offset:3072 lds
ds_write_b128 v137, v[28:31] offset:3072
s_waitcnt vmcnt(7)
v_mfma_f32_32x32x16_bf16 v[32:47], a[228:231], v[160:163], v[32:47]
ds_write_b128 v139, v[48:51] offset:0
v_mfma_f32_32x32x16_bf16 v[0:15], a[224:227], v[160:163], v[0:15]
s_add_u32 m0, s48, 4096
s_add_u32 s49, s49, 35840
s_cmp_eq_u32 s49, s65
s_cselect_b32 s49, s64, s49
buffer_load_dwordx4 v244, s[24:27], s49 offen offset:0 lds
ds_write_b128 v139, v[52:55] offset:1024
s_waitcnt vmcnt(7)
v_mfma_f32_32x32x16_bf16 v[32:47], a[236:239], v[164:167], v[32:47]
ds_write_b128 v139, v[56:59] offset:2048
v_mfma_f32_32x32x16_bf16 v[0:15], a[232:235], v[164:167], v[0:15]
s_add_u32 s49, s49, 31744
buffer_load_dwordx4 v244, s[24:27], s49 offen offset:1024 lds
ds_write_b128 v139, v[60:63] offset:3072
s_waitcnt vmcnt(7)
v_mfma_f32_32x32x16_bf16 v[32:47], a[244:247], v[168:171], v[32:47]
s_waitcnt vmcnt(6)
v_mfma_f32_32x32x16_bf16 v[32:47], a[252:255], v[172:175], v[32:47]
s_add_u32 s49, s49, 31744
buffer_load_dwordx4 v244, s[24:27], s49 offen offset:2048 lds
v_mfma_f32_32x32x16_bf16 v[0:15], a[240:243], v[168:171], v[0:15]
v_mfma_f32_32x32x16_bf16 v[0:15], a[248:251], v[172:175], v[0:15]
s_add_u32 s49, s49, 31744
buffer_load_dwordx4 v244, s[24:27], s49 offen offset:3072 lds
s_nop 5
ds_write_b128 v138, v[32:35] offset:0
ds_write_b128 v138, v[36:39] offset:1024
ds_write_b128 v138, v[40:43] offset:2048
ds_write_b128 v138, v[44:47] offset:3072
s_waitcnt lgkmcnt(0)
s_barrier
ds_read_b128 v[16:19], v136 offset:0
ds_read_b128 v[32:35], v136 offset:4096
ds_read_b128 v[48:51], v136 offset:8192
ds_read_b128 v[20:23], v136 offset:1024
ds_read_b128 v[36:39], v136 offset:5120
ds_read_b128 v[52:55], v136 offset:9216
ds_read_b128 v[24:27], v136 offset:2048
ds_read_b128 v[40:43], v136 offset:6144
ds_read_b128 v[56:59], v136 offset:10240
ds_read_b128 v[28:31], v136 offset:3072
ds_read_b128 v[44:47], v136 offset:7168
ds_read_b128 v[60:63], v136 offset:11264
s_mul_i32 s51, s56, 176
s_lshl_b32 s52, s55, 3
s_add_u32 s51, s51, s52
s_and_b32 s52, s69, 2
s_add_u32 s52, s52, s69
s_add_u32 s51, s51, s52
s_lshl_b32 s51, s51, 10
s_add_u32 s51, s51, 3145728
s_mov_b32 s60, 0xbdd2d3e8
v_mov_b32 v68, 0xc0135761
s_waitcnt lgkmcnt(11)
v_pk_add_f32 v[0:1], v[0:1], v[16:17]
v_pk_add_f32 v[2:3], v[2:3], v[18:19]
s_waitcnt lgkmcnt(9)
v_pk_add_f32 v[32:33], v[32:33], v[48:49]
v_pk_add_f32 v[34:35], v[34:35], v[50:51]
v_pk_add_f32 v[0:1], v[0:1], v[32:33]
v_pk_add_f32 v[2:3], v[2:3], v[34:35]
s_waitcnt lgkmcnt(8)
v_pk_add_f32 v[4:5], v[4:5], v[20:21]
v_pk_add_f32 v[6:7], v[6:7], v[22:23]
s_waitcnt lgkmcnt(6)
v_pk_add_f32 v[36:37], v[36:37], v[52:53]
v_pk_add_f32 v[38:39], v[38:39], v[54:55]
v_pk_add_f32 v[4:5], v[4:5], v[36:37]
v_pk_add_f32 v[6:7], v[6:7], v[38:39]
v_pk_mul_f32 v[80:81], v[0:1], v[0:1]
v_pk_mul_f32 v[82:83], v[2:3], v[2:3]
v_pk_mul_f32 v[84:85], v[4:5], v[4:5]
v_pk_mul_f32 v[86:87], v[6:7], v[6:7]
v_pk_fma_f32 v[80:81], v[80:81], s[60:61], v[68:69] op_sel_hi:[1,0,0]
v_pk_fma_f32 v[82:83], v[82:83], s[60:61], v[68:69] op_sel_hi:[1,0,0]
v_pk_fma_f32 v[84:85], v[84:85], s[60:61], v[68:69] op_sel_hi:[1,0,0]
v_pk_fma_f32 v[86:87], v[86:87], s[60:61], v[68:69] op_sel_hi:[1,0,0]
v_pk_mul_f32 v[80:81], v[80:81], v[0:1]
v_pk_mul_f32 v[82:83], v[82:83], v[2:3]
v_pk_mul_f32 v[84:85], v[84:85], v[4:5]
v_pk_mul_f32 v[86:87], v[86:87], v[6:7]
v_exp_f32 v80, v80
v_exp_f32 v81, v81
v_exp_f32 v82, v82
v_exp_f32 v83, v83
v_exp_f32 v84, v84
v_exp_f32 v85, v85
v_exp_f32 v86, v86
v_exp_f32 v87, v87
v_pk_add_f32 v[80:81], v[80:81], 1.0 op_sel_hi:[1,0]
v_pk_add_f32 v[82:83], v[82:83], 1.0 op_sel_hi:[1,0]
v_pk_add_f32 v[84:85], v[84:85], 1.0 op_sel_hi:[1,0]
v_pk_add_f32 v[86:87], v[86:87], 1.0 op_sel_hi:[1,0]
v_rcp_f32 v80, v80
v_rcp_f32 v81, v81
v_rcp_f32 v82, v82
v_rcp_f32 v83, v83
v_rcp_f32 v84, v84
v_rcp_f32 v85, v85
v_rcp_f32 v86, v86
v_rcp_f32 v87, v87
v_pk_mul_f32 v[80:81], v[80:81], v[0:1]
v_pk_mul_f32 v[82:83], v[82:83], v[2:3]
v_pk_mul_f32 v[84:85], v[84:85], v[4:5]
v_pk_mul_f32 v[86:87], v[86:87], v[6:7]
v_cvt_pk_bf16_f32 v72, v80, v81
v_cvt_pk_bf16_f32 v73, v82, v83
v_cvt_pk_bf16_f32 v74, v84, v85
v_cvt_pk_bf16_f32 v75, v86, v87
buffer_store_dwordx4 v[72:75], v241, s[12:15], s51 offen offset:0 sc1
s_waitcnt lgkmcnt(5)
v_pk_add_f32 v[8:9], v[8:9], v[24:25]
v_pk_add_f32 v[10:11], v[10:11], v[26:27]
s_waitcnt lgkmcnt(3)
v_pk_add_f32 v[40:41], v[40:41], v[56:57]
v_pk_add_f32 v[42:43], v[42:43], v[58:59]
v_pk_add_f32 v[8:9], v[8:9], v[40:41]
v_pk_add_f32 v[10:11], v[10:11], v[42:43]
s_waitcnt lgkmcnt(2)
v_pk_add_f32 v[12:13], v[12:13], v[28:29]
v_pk_add_f32 v[14:15], v[14:15], v[30:31]
s_waitcnt lgkmcnt(0)
v_pk_add_f32 v[44:45], v[44:45], v[60:61]
v_pk_add_f32 v[46:47], v[46:47], v[62:63]
v_pk_add_f32 v[12:13], v[12:13], v[44:45]
v_pk_add_f32 v[14:15], v[14:15], v[46:47]
v_pk_mul_f32 v[88:89], v[8:9], v[8:9]
v_pk_mul_f32 v[90:91], v[10:11], v[10:11]
v_pk_mul_f32 v[92:93], v[12:13], v[12:13]
v_pk_mul_f32 v[94:95], v[14:15], v[14:15]
v_pk_fma_f32 v[88:89], v[88:89], s[60:61], v[68:69] op_sel_hi:[1,0,0]
v_pk_fma_f32 v[90:91], v[90:91], s[60:61], v[68:69] op_sel_hi:[1,0,0]
v_pk_fma_f32 v[92:93], v[92:93], s[60:61], v[68:69] op_sel_hi:[1,0,0]
v_pk_fma_f32 v[94:95], v[94:95], s[60:61], v[68:69] op_sel_hi:[1,0,0]
v_pk_mul_f32 v[88:89], v[88:89], v[8:9]
v_pk_mul_f32 v[90:91], v[90:91], v[10:11]
v_pk_mul_f32 v[92:93], v[92:93], v[12:13]
v_pk_mul_f32 v[94:95], v[94:95], v[14:15]
v_exp_f32 v88, v88
v_exp_f32 v89, v89
v_exp_f32 v90, v90
v_exp_f32 v91, v91
v_exp_f32 v92, v92
v_exp_f32 v93, v93
v_exp_f32 v94, v94
v_exp_f32 v95, v95
v_pk_add_f32 v[88:89], v[88:89], 1.0 op_sel_hi:[1,0]
v_pk_add_f32 v[90:91], v[90:91], 1.0 op_sel_hi:[1,0]
v_pk_add_f32 v[92:93], v[92:93], 1.0 op_sel_hi:[1,0]
v_pk_add_f32 v[94:95], v[94:95], 1.0 op_sel_hi:[1,0]
v_rcp_f32 v88, v88
v_rcp_f32 v89, v89
v_rcp_f32 v90, v90
v_rcp_f32 v91, v91
v_rcp_f32 v92, v92
v_rcp_f32 v93, v93
v_rcp_f32 v94, v94
v_rcp_f32 v95, v95
v_pk_mul_f32 v[88:89], v[88:89], v[8:9]
v_pk_mul_f32 v[90:91], v[90:91], v[10:11]
v_pk_mul_f32 v[92:93], v[92:93], v[12:13]
v_pk_mul_f32 v[94:95], v[94:95], v[14:15]
v_cvt_pk_bf16_f32 v76, v88, v89
v_cvt_pk_bf16_f32 v77, v90, v91
v_cvt_pk_bf16_f32 v78, v92, v93
v_cvt_pk_bf16_f32 v79, v94, v95
buffer_store_dwordx4 v[76:79], v241, s[12:15], s51 offen offset:2048 sc1
s_bitcmp1_b32 s44, 25
s_cbranch_scc0 EP_17
s_mov_b32 s53, 0
s_cmp_lg_u32 s69, 0
s_cbranch_scc1 FL_18
v_mov_b32 v250, s46
v_mov_b32 v251, s47
s_mov_b64 exec, 1
buffer_store_dwordx2 v[250:251], off, s[12:15], s53 sc1
s_mov_b64 exec, -1
FL_18:
EP_17:
s_mul_i32 s53, s56, 22
s_add_u32 s53, s53, s55
s_lshl_b32 s53, s53, 7
s_add_u32 s53, s53, 36864
s_add_u32 m0, s48, 8192
s_add_u32 s49, s49, 35840
s_cmp_eq_u32 s49, s65
s_cselect_b32 s49, s64, s49
buffer_load_dwordx4 v244, s[24:27], s49 offen offset:0 lds
s_add_u32 s49, s49, 31744
buffer_load_dwordx4 v244, s[24:27], s49 offen offset:1024 lds
s_add_u32 s49, s49, 31744
buffer_load_dwordx4 v244, s[24:27], s49 offen offset:2048 lds
s_add_u32 s49, s49, 31744
buffer_load_dwordx4 v244, s[24:27], s49 offen offset:3072 lds
s_bfe_u32 s54, s69, 0x10000
s_lshl_b32 s54, s54, 6
v_lshrrev_b32 v248, 5, v240
v_lshl_add_u32 v248, v248, 9, s48
v_xad_u32 v247, v242, s54, v248
v_xor_b32 v248, 64, v247
s_waitcnt vmcnt(12)
ds_read_b64_tr_b16 a[0:1], v247 offset:0
ds_read_b64_tr_b16 a[2:3], v247 offset:1024
s_add_u32 m0, s48, 12288
s_add_u32 s49, s49, 35840
s_cmp_eq_u32 s49, s65
s_cselect_b32 s49, s64, s49
buffer_load_dwordx4 v244, s[24:27], s49 offen offset:0 lds
s_and_b32 s51, s69, 1
s_lshl_b32 s51, s51, 5
s_lshl_b32 s52, s57, 6
s_add_u32 s51, s51, s52
v_and_b32 v254, 31, v240
v_add_u32 v254, s51, v254
v_lshlrev_b32 v255, 1, v254
buffer_load_ushort v0, v255, s[36:39], 0 offen
v_lshrrev_b32 v2, 3, v254
v_and_b32 v3, 7, v254
v_lshlrev_b32 v3, 2, v3
v_lshl_add_u32 v255, v2, 7, v3
s_mul_i32 s51, s69, 22
s_lshr_b32 s52, s51, 2
s_mul_i32 s67, s58, 22
s_add_u32 s52, s52, s67
s_add_u32 s52, s52, 288
v_min_u32 v2, 5, v240
v_add_lshl_u32 v17, v2, s52, 7
s_mul_i32 s67, s58, 88
s_add_u32 s51, s51, s67
s_lshl_b32 s51, s51, 11
s_add_u32 s59, s51, 3145728
s_mov_b32 s66, s59
s_add_u32 s68, s59, 45056
s_bfe_u32 s51, s45, 0x40004
s_mul_i32 s51, s51, 4096
s_add_u32 s59, s59, s51
s_bfe_u32 s51, s69, 0x10001
s_lshl_b32 s51, s51, 10
v_add_u32 v245, s51, v241
v_xor_b32 v246, 0x400, v245
v_xor_b32 v16, 0x200, v255
s_bfe_u32 s51, s44, 0x40011
s_bfe_u32 s52, s44, 0x40015
s_sub_u32 s52, s52, s51
s_lshl_b32 s52, s52, 2
s_lshl_b32 s51, s51, 3
s_lshl_b32 s67, s69, 1
s_add_u32 s51, s51, s67
s_add_u32 s51, s51, 480
v_and_b32 v249, 2, v240
v_and_b32 v2, 1, v240
v_mad_u32_u24 v249, v249, s52, v2
v_add_lshl_u32 v249, v249, s51, 7
s_waitcnt vmcnt(6)
s_barrier
s_cmp_lg_u32 s69, 0
s_cbranch_scc1 FL_19
v_mov_b32 v250, s46
v_mov_b32 v251, s47
s_mov_b64 exec, 1
buffer_store_dwordx2 v[250:251], off, s[12:15], s53 sc1
s_mov_b64 exec, -1
FL_19:
ds_read_b64_tr_b16 a[4:5], v248 offset:0
ds_read_b64_tr_b16 a[6:7], v248 offset:1024
s_add_u32 s49, s49, 31744
buffer_load_dwordx4 v244, s[24:27], s49 offen offset:1024 lds
s_add_u32 s49, s49, 31744
buffer_load_dwordx4 v244, s[24:27], s49 offen offset:2048 lds
s_add_u32 s49, s49, 31744
buffer_load_dwordx4 v244, s[24:27], s49 offen offset:3072 lds
s_add_u32 m0, s48, 16384
s_add_u32 s49, s49, 35840
s_cmp_eq_u32 s49, s65
s_cselect_b32 s49, s64, s49
buffer_load_dwordx4 v244, s[24:27], s49 offen offset:0 lds
s_add_u32 s49, s49, 31744
buffer_load_dwordx4 v244, s[24:27], s49 offen offset:1024 lds
s_add_u32 s49, s49, 31744
buffer_load_dwordx4 v244, s[24:27], s49 offen offset:2048 lds
s_add_u32 s49, s49, 31744
buffer_load_dwordx4 v244, s[24:27], s49 offen offset:3072 lds
s_add_u32 m0, s48, 20480
s_add_u32 s49, s49, 35840
s_cmp_eq_u32 s49, s65
s_cselect_b32 s49, s64, s49
buffer_load_dwordx4 v244, s[24:27], s49 offen offset:0 lds
s_add_u32 s49, s49, 31744
buffer_load_dwordx4 v244, s[24:27], s49 offen offset:1024 lds
s_add_u32 s49, s49, 31744
buffer_load_dwordx4 v244, s[24:27], s49 offen offset:2048 lds
s_add_u32 s49, s49, 31744
buffer_load_dwordx4 v244, s[24:27], s49 offen offset:3072 lds
s_waitcnt lgkmcnt(0)
s_add_u32 m0, s48, 0
s_add_u32 s49, s49, 35840
s_cmp_eq_u32 s49, s65
s_cselect_b32 s49, s64, s49
buffer_load_dwordx4 v244, s[24:27], s49 offen offset:0 lds
s_add_u32 s49, s49, 31744
buffer_load_dwordx4 v244, s[24:27], s49 offen offset:1024 lds
ds_read_b64_tr_b16 a[8:9], v247 offset:2048
ds_read_b64_tr_b16 a[10:11], v247 offset:3072
ds_read_b64_tr_b16 a[12:13], v248 offset:2048
ds_read_b64_tr_b16 a[14:15], v248 offset:3072
ds_read_b64_tr_b16 a[16:17], v247 offset:4096
ds_read_b64_tr_b16 a[18:19], v247 offset:5120
ds_read_b64_tr_b16 a[20:21], v248 offset:4096
ds_read_b64_tr_b16 a[22:23], v248 offset:5120
s_waitcnt lgkmcnt(4)
s_add_u32 s49, s49, 31744
buffer_load_dwordx4 v244, s[24:27], s49 offen offset:2048 lds
s_add_u32 s49, s49, 31744
buffer_load_dwordx4 v244, s[24:27], s49 offen offset:3072 lds
s_waitcnt lgkmcnt(0)
s_add_u32 m0, s48, 4096
s_add_u32 s49, s49, 35840
s_cmp_eq_u32 s49, s65
s_cselect_b32 s49, s64, s49
buffer_load_dwordx4 v244, s[24:27], s49 offen offset:0 lds
s_add_u32 s49, s49, 31744
buffer_load_dwordx4 v244, s[24:27], s49 offen offset:1024 lds
ds_read_b64_tr_b16 a[24:25], v247 offset:6144
ds_read_b64_tr_b16 a[26:27], v247 offset:7168
ds_read_b64_tr_b16 a[28:29], v248 offset:6144
ds_read_b64_tr_b16 a[30:31], v248 offset:7168
s_waitcnt vmcnt(21)
ds_read_b64_tr_b16 a[32:33], v247 offset:8192
ds_read_b64_tr_b16 a[34:35], v247 offset:9216
ds_read_b64_tr_b16 a[36:37], v248 offset:8192
ds_read_b64_tr_b16 a[38:39], v248 offset:9216
s_waitcnt lgkmcnt(4)
s_add_u32 s49, s49, 31744
buffer_load_dwordx4 v244, s[24:27], s49 offen offset:2048 lds
s_add_u32 s49, s49, 31744
buffer_load_dwordx4 v244, s[24:27], s49 offen offset:3072 lds
s_waitcnt lgkmcnt(0)
s_add_u32 m0, s48, 8192
s_add_u32 s49, s49, 35840
s_cmp_eq_u32 s49, s65
s_cselect_b32 s49, s64, s49
buffer_load_dwordx4 v244, s[24:27], s49 offen offset:0 lds
s_add_u32 s49, s49, 31744
buffer_load_dwordx4 v244, s[24:27], s49 offen offset:1024 lds
s_waitcnt vmcnt(23)
ds_read_b64_tr_b16 a[40:41], v247 offset:10240
ds_read_b64_tr_b16 a[42:43], v247 offset:11264
ds_read_b64_tr_b16 a[44:45], v248 offset:10240
ds_read_b64_tr_b16 a[46:47], v248 offset:11264
s_waitcnt vmcnt(20)
ds_read_b64_tr_b16 a[48:49], v247 offset:12288
ds_read_b64_tr_b16 a[50:51], v247 offset:13312
ds_read_b64_tr_b16 a[52:53], v248 offset:12288
ds_read_b64_tr_b16 a[54:55], v248 offset:13312
s_waitcnt lgkmcnt(4)
s_add_u32 s49, s49, 31744
buffer_load_dwordx4 v244, s[24:27], s49 offen offset:2048 lds
s_add_u32 s49, s49, 31744
buffer_load_dwordx4 v244, s[24:27], s49 offen offset:3072 lds
s_waitcnt lgkmcnt(0)
s_add_u32 m0, s48, 12288
s_add_u32 s49, s49, 35840
s_cmp_eq_u32 s49, s65
s_cselect_b32 s49, s64, s49
buffer_load_dwordx4 v244, s[24:27], s49 offen offset:0 lds
s_add_u32 s49, s49, 31744
buffer_load_dwordx4 v244, s[24:27], s49 offen offset:1024 lds
s_waitcnt vmcnt(22)
ds_read_b64_tr_b16 a[56:57], v247 offset:14336
ds_read_b64_tr_b16 a[58:59], v247 offset:15360
ds_read_b64_tr_b16 a[60:61], v248 offset:14336
ds_read_b64_tr_b16 a[62:63], v248 offset:15360
s_waitcnt vmcnt(20)
ds_read_b64_tr_b16 a[64:65], v247 offset:16384
ds_read_b64_tr_b16 a[66:67], v247 offset:17408
ds_read_b64_tr_b16 a[68:69], v248 offset:16384
ds_read_b64_tr_b16 a[70:71], v248 offset:17408
s_waitcnt lgkmcnt(4)
s_add_u32 s49, s49, 31744
buffer_load_dwordx4 v244, s[24:27], s49 offen offset:2048 lds
s_add_u32 s49, s49, 31744
buffer_load_dwordx4 v244, s[24:27], s49 offen offset:3072 lds
s_waitcnt lgkmcnt(0)
s_add_u32 m0, s48, 16384
s_add_u32 s49, s49, 35840
s_cmp_eq_u32 s49, s65
s_cselect_b32 s49, s64, s49
buffer_load_dwordx4 v244, s[24:27], s49 offen offset:0 lds
s_add_u32 s49, s49, 31744
buffer_load_dwordx4 v244, s[24:27], s49 offen offset:1024 lds
s_waitcnt vmcnt(22)
ds_read_b64_tr_b16 a[72:73], v247 offset:18432
ds_read_b64_tr_b16 a[74:75], v247 offset:19456
ds_read_b64_tr_b16 a[76:77], v248 offset:18432
ds_read_b64_tr_b16 a[78:79], v248 offset:19456
s_waitcnt vmcnt(20)
ds_read_b64_tr_b16 a[80:81], v247 offset:20480
ds_read_b64_tr_b16 a[82:83], v247 offset:21504
ds_read_b64_tr_b16 a[84:85], v248 offset:20480
ds_read_b64_tr_b16 a[86:87], v248 offset:21504
s_waitcnt lgkmcnt(4)
s_add_u32 s49, s49, 31744
buffer_load_dwordx4 v244, s[24:27], s49 offen offset:2048 lds
s_add_u32 s49, s49, 31744
buffer_load_dwordx4 v244, s[24:27], s49 offen offset:3072 lds
s_waitcnt lgkmcnt(0)
v_lshrrev_b32 v244, 3, v240
v_mul_u32_u24 v244, 1024, v244
v_add_u32 v244, v244, v243
s_add_u32 m0, s48, 20480
s_nop 0
buffer_load_dwordx4 v244, s[28:31], s50 offen offset:0 lds
s_add_u32 s50, s50, 7168
buffer_load_dwordx4 v244, s[28:31], s50 offen offset:1024 lds
buffer_load_dwordx2 v[250:251], v17, s[12:15], 0 offen sc1
s_waitcnt vmcnt(23)
ds_read_b64_tr_b16 a[88:89], v247 offset:22528
ds_read_b64_tr_b16 a[90:91], v247 offset:23552
ds_read_b64_tr_b16 a[92:93], v248 offset:22528
ds_read_b64_tr_b16 a[94:95], v248 offset:23552
s_waitcnt lgkmcnt(0)
s_add_u32 s50, s50, 7168
buffer_load_dwordx4 v244, s[28:31], s50 offen offset:2048 lds
s_add_u32 s50, s50, 7168
buffer_load_dwordx4 v244, s[28:31], s50 offen offset:3072 lds
s_waitcnt vmcnt(23)
ds_read_b64_tr_b16 a[96:97], v247 offset:0
ds_read_b64_tr_b16 a[98:99], v247 offset:1024
ds_read_b64_tr_b16 a[100:101], v248 offset:0
ds_read_b64_tr_b16 a[102:103], v248 offset:1024
s_waitcnt vmcnt(21)
ds_read_b64_tr_b16 a[104:105], v247 offset:2048
ds_read_b64_tr_b16 a[106:107], v247 offset:3072
ds_read_b64_tr_b16 a[108:109], v248 offset:2048
ds_read_b64_tr_b16 a[110:111], v248 offset:3072
s_waitcnt lgkmcnt(4)
s_add_u32 m0, s48, 0
s_add_u32 s50, s50, 11264
s_cmp_eq_u32 s50, s1
s_cselect_b32 s50, s0, s50
buffer_load_dwordx4 v244, s[28:31], s50 offen offset:0 lds
s_add_u32 s50, s50, 7168
buffer_load_dwordx4 v244, s[28:31], s50 offen offset:1024 lds
s_waitcnt lgkmcnt(0)
s_add_u32 s50, s50, 7168
buffer_load_dwordx4 v244, s[28:31], s50 offen offset:2048 lds
s_add_u32 s50, s50, 7168
buffer_load_dwordx4 v244, s[28:31], s50 offen offset:3072 lds
s_waitcnt vmcnt(23)
ds_read_b64_tr_b16 a[112:113], v247 offset:4096
ds_read_b64_tr_b16 a[114:115], v247 offset:5120
ds_read_b64_tr_b16 a[116:117], v248 offset:4096
ds_read_b64_tr_b16 a[118:119], v248 offset:5120
s_waitcnt vmcnt(21)
ds_read_b64_tr_b16 a[120:121], v247 offset:6144
ds_read_b64_tr_b16 a[122:123], v247 offset:7168
ds_read_b64_tr_b16 a[124:125], v248 offset:6144
ds_read_b64_tr_b16 a[126:127], v248 offset:7168
s_waitcnt lgkmcnt(4)
s_add_u32 m0, s48, 4096
s_add_u32 s50, s50, 11264
s_cmp_eq_u32 s50, s1
s_cselect_b32 s50, s0, s50
buffer_load_dwordx4 v244, s[28:31], s50 offen offset:0 lds
s_add_u32 s50, s50, 7168
buffer_load_dwordx4 v244, s[28:31], s50 offen offset:1024 lds
s_waitcnt lgkmcnt(0)
s_add_u32 s50, s50, 7168
buffer_load_dwordx4 v244, s[28:31], s50 offen offset:2048 lds
s_add_u32 s50, s50, 7168
buffer_load_dwordx4 v244, s[28:31], s50 offen offset:3072 lds
s_waitcnt vmcnt(23)
ds_read_b64_tr_b16 a[128:129], v247 offset:8192
ds_read_b64_tr_b16 a[130:131], v247 offset:9216
ds_read_b64_tr_b16 a[132:133], v248 offset:8192
ds_read_b64_tr_b16 a[134:135], v248 offset:9216
s_waitcnt vmcnt(21)
ds_read_b64_tr_b16 a[136:137], v247 offset:10240
ds_read_b64_tr_b16 a[138:139], v247 offset:11264
ds_read_b64_tr_b16 a[140:141], v248 offset:10240
ds_read_b64_tr_b16 a[142:143], v248 offset:11264
s_waitcnt vmcnt(19)
ds_read_b64_tr_b16 a[144:145], v247 offset:12288
ds_read_b64_tr_b16 a[146:147], v247 offset:13312
ds_read_b64_tr_b16 a[148:149], v248 offset:12288
ds_read_b64_tr_b16 a[150:151], v248 offset:13312
s_waitcnt vmcnt(17)
ds_read_b64_tr_b16 a[152:153], v247 offset:14336
ds_read_b64_tr_b16 a[154:155], v247 offset:15360
ds_read_b64_tr_b16 a[156:157], v248 offset:14336
ds_read_b64_tr_b16 a[158:159], v248 offset:15360
s_waitcnt vmcnt(15)
ds_read_b64_tr_b16 a[160:161], v247 offset:16384
ds_read_b64_tr_b16 a[162:163], v247 offset:17408
ds_read_b64_tr_b16 a[164:165], v248 offset:16384
ds_read_b64_tr_b16 a[166:167], v248 offset:17408
s_waitcnt vmcnt(13)
ds_read_b64_tr_b16 a[168:169], v247 offset:18432
ds_read_b64_tr_b16 a[170:171], v247 offset:19456
ds_read_b64_tr_b16 a[172:173], v248 offset:18432
ds_read_b64_tr_b16 a[174:175], v248 offset:19456
s_bfe_u32 s54, s69, 0x10001
s_lshl_b32 s54, s54, 6
v_lshrrev_b32 v248, 5, v240
v_lshl_add_u32 v248, v248, 10, s48
v_xad_u32 v247, v242, s54, v248
v_xor_b32 v248, 64, v247
s_waitcnt vmcnt(11)
ds_read_b64_tr_b16 a[176:177], v247 offset:20480
ds_read_b64_tr_b16 a[178:179], v247 offset:20992
ds_read_b64_tr_b16 a[180:181], v248 offset:20480
ds_read_b64_tr_b16 a[182:183], v248 offset:20992
v_lshlrev_b32 v0, 16, v0
v_mov_b32 v1, v0
v_pk_mov_b32 v[2:3], v[0:1], v[0:1]
v_pk_mov_b32 v[4:5], v[0:1], v[0:1]
v_pk_mov_b32 v[6:7], v[0:1], v[0:1]
v_pk_mov_b32 v[8:9], v[0:1], v[0:1]
v_pk_mov_b32 v[10:11], v[0:1], v[0:1]
v_pk_mov_b32 v[12:13], v[0:1], v[0:1]
v_pk_mov_b32 v[14:15], v[0:1], v[0:1]
s_mov_b32 s67, 131072
s_waitcnt vmcnt(10)
v_cmp_ne_u64 vcc, s[46:47], v[250:251]
s_mov_b32 s54, 0
s_nop 0
s_cbranch_vccz POLLD_21
POLL_20:
buffer_load_dwordx2 v[250:251], v17, s[12:15], 0 offen sc1
s_waitcnt vmcnt(0)
v_cmp_ne_u64 vcc, s[46:47], v[250:251]
s_nop 1
s_cbranch_vccz POLLD_21
s_add_u32 s54, s54, 1
s_cmp_lt_u32 s54, 200000
s_sleep 1
s_cbranch_scc1 POLL_20
POLLD_21:
buffer_load_dwordx4 v[64:67], v245, s[12:15], s59 offen offset:0 sc1
buffer_load_dwordx4 v[68:71], v246, s[12:15], s59 offen offset:0 sc1
buffer_load_dwordx4 v[72:75], v245, s[12:15], s59 offen offset:2048 sc1
buffer_load_dwordx4 v[76:79], v246, s[12:15], s59 offen offset:2048 sc1
buffer_load_dword v252, v255, s[12:15], s67 offen sc1
buffer_load_dword v253, v16, s[12:15], s67 offen sc1
s_add_u32 s59, s59, 4096
s_cmp_eq_u32 s59, s68
s_cselect_b32 s59, s66, s59
buffer_load_dwordx4 v[80:83], v245, s[12:15], s59 offen offset:0 sc1
buffer_load_dwordx4 v[84:87], v246, s[12:15], s59 offen offset:0 sc1
buffer_load_dwordx4 v[88:91], v245, s[12:15], s59 offen offset:2048 sc1
buffer_load_dwordx4 v[92:95], v246, s[12:15], s59 offen offset:2048 sc1
s_waitcnt vmcnt(9)
v_mfma_f32_32x32x16_bf16 v[16:31], v[64:67], a[4:7], 0
s_waitcnt lgkmcnt(15)
s_add_u32 m0, s48, 8192
s_add_u32 s50, s50, 11264
s_cmp_eq_u32 s50, s1
s_cselect_b32 s50, s0, s50
buffer_load_dwordx4 v244, s[28:31], s50 offen offset:0 lds
ds_read_b64_tr_b16 a[184:185], v247 offset:22528
s_waitcnt vmcnt(9)
v_mfma_f32_32x32x16_bf16 v[48:63], v[68:71], a[4:7], 0
s_add_u32 s59, s59, 4096
s_cmp_eq_u32 s59, s68
s_cselect_b32 s59, s66, s59
buffer_load_dwordx4 v[96:99], v245, s[12:15], s59 offen offset:0 sc1
v_mfma_f32_32x32x16_bf16 v[32:47], v[68:71], a[0:3], 0
ds_read_b64_tr_b16 a[186:187], v247 offset:23040
ds_read_b64_tr_b16 a[188:189], v248 offset:22528
v_mfma_f32_32x32x16_bf16 v[0:15], v[64:67], a[0:3], v[0:15]
buffer_load_dwordx4 v[100:103], v246, s[12:15], s59 offen offset:0 sc1
s_waitcnt vmcnt(10)
v_mfma_f32_32x32x16_bf16 v[0:15], v[72:75], a[8:11], v[0:15]
s_add_u32 s50, s50, 7168
buffer_load_dwordx4 v244, s[28:31], s50 offen offset:1024 lds
ds_read_b64_tr_b16 a[190:191], v248 offset:23040
v_mfma_f32_32x32x16_bf16 v[16:31], v[72:75], a[12:15], v[16:31]
buffer_load_dwordx4 v[104:107], v245, s[12:15], s59 offen offset:2048 sc1
s_waitcnt vmcnt(11)
v_mfma_f32_32x32x16_bf16 v[48:63], v[76:79], a[12:15], v[48:63]
ds_read_b64_tr_b16 a[192:193], v247 offset:0
ds_read_b64_tr_b16 a[194:195], v247 offset:512
v_mfma_f32_32x32x16_bf16 v[32:47], v[76:79], a[8:11], v[32:47]
buffer_load_dwordx4 v[108:111], v246, s[12:15], s59 offen offset:2048 sc1
s_waitcnt vmcnt(9)
v_mfma_f32_32x32x16_bf16 v[0:15], v[80:83], a[16:19], v[0:15]
s_add_u32 s50, s50, 7168
buffer_load_dwordx4 v244, s[28:31], s50 offen offset:2048 lds
ds_read_b64_tr_b16 a[196:197], v248 offset:0
v_mfma_f32_32x32x16_bf16 v[16:31], v[80:83], a[20:23], v[16:31]
s_add_u32 s59, s59, 4096
s_cmp_eq_u32 s59, s68
s_cselect_b32 s59, s66, s59
buffer_load_dwordx4 v[112:115], v245, s[12:15], s59 offen offset:0 sc1
s_waitcnt vmcnt(10)
v_mfma_f32_32x32x16_bf16 v[48:63], v[84:87], a[20:23], v[48:63]
ds_read_b64_tr_b16 a[198:199], v248 offset:512
ds_read_b64_tr_b16 a[200:201], v247 offset:2048
v_mfma_f32_32x32x16_bf16 v[32:47], v[84:87], a[16:19], v[32:47]
buffer_load_dwordx4 v[116:119], v246, s[12:15], s59 offen offset:0 sc1
s_waitcnt vmcnt(10)
v_mfma_f32_32x32x16_bf16 v[0:15], v[88:91], a[24:27], v[0:15]
s_add_u32 s50, s50, 7168
buffer_load_dwordx4 v244, s[28:31], s50 offen offset:3072 lds
ds_read_b64_tr_b16 a[202:203], v247 offset:2560
v_mfma_f32_32x32x16_bf16 v[16:31], v[88:91], a[28:31], v[16:31]
buffer_load_dwordx4 v[120:123], v245, s[12:15], s59 offen offset:2048 sc1
s_waitcnt vmcnt(11)
v_mfma_f32_32x32x16_bf16 v[48:63], v[92:95], a[28:31], v[48:63]
ds_read_b64_tr_b16 a[204:205], v248 offset:2048
ds_read_b64_tr_b16 a[206:207], v248 offset:2560
v_mfma_f32_32x32x16_bf16 v[32:47], v[92:95], a[24:27], v[32:47]
buffer_load_dwordx4 v[124:127], v246, s[12:15], s59 offen offset:2048 sc1
s_waitcnt vmcnt(10)
v_mfma_f32_32x32x16_bf16 v[0:15], v[96:99], a[32:35], v[0:15]
ds_read_b64_tr_b16 a[208:209], v247 offset:4096
ds_read_b64_tr_b16 a[210:211], v247 offset:4608
v_mfma_f32_32x32x16_bf16 v[16:31], v[96:99], a[36:39], v[16:31]
s_add_u32 s59, s59, 4096
s_cmp_eq_u32 s59, s68
s_cselect_b32 s59, s66, s59
buffer_load_dwordx4 v[128:131], v245, s[12:15], s59 offen offset:0 sc1
s_waitcnt vmcnt(10)
v_mfma_f32_32x32x16_bf16 v[48:63], v[100:103], a[36:39], v[48:63]
ds_read_b64_tr_b16 a[212:213], v248 offset:4096
ds_read_b64_tr_b16 a[214:215], v248 offset:4608
v_mfma_f32_32x32x16_bf16 v[32:47], v[100:103], a[32:35], v[32:47]
buffer_load_dwordx4 v[132:135], v246, s[12:15], s59 offen offset:0 sc1
s_waitcnt vmcnt(9)
v_mfma_f32_32x32x16_bf16 v[0:15], v[104:107], a[40:43], v[0:15]
ds_read_b64_tr_b16 a[216:217], v247 offset:6144
ds_read_b64_tr_b16 a[218:219], v247 offset:6656
v_mfma_f32_32x32x16_bf16 v[16:31], v[104:107], a[44:47], v[16:31]
buffer_load_dwordx4 v[136:139], v245, s[12:15], s59 offen offset:2048 sc1
s_waitcnt vmcnt(9)
v_mfma_f32_32x32x16_bf16 v[48:63], v[108:111], a[44:47], v[48:63]
ds_read_b64_tr_b16 a[220:221], v248 offset:6144
ds_read_b64_tr_b16 a[222:223], v248 offset:6656
v_mfma_f32_32x32x16_bf16 v[32:47], v[108:111], a[40:43], v[32:47]
buffer_load_dwordx4 v[140:143], v246, s[12:15], s59 offen offset:2048 sc1
s_waitcnt vmcnt(8)
v_mfma_f32_32x32x16_bf16 v[0:15], v[112:115], a[48:51], v[0:15]
ds_read_b64_tr_b16 a[224:225], v247 offset:8192
ds_read_b64_tr_b16 a[226:227], v247 offset:8704
v_mfma_f32_32x32x16_bf16 v[16:31], v[112:115], a[52:55], v[16:31]
s_add_u32 s59, s59, 4096
s_cmp_eq_u32 s59, s68
s_cselect_b32 s59, s66, s59
buffer_load_dwordx4 v[144:147], v245, s[12:15], s59 offen offset:0 sc1
s_waitcnt vmcnt(8)
v_mfma_f32_32x32x16_bf16 v[48:63], v[116:119], a[52:55], v[48:63]
ds_read_b64_tr_b16 a[228:229], v248 offset:8192
ds_read_b64_tr_b16 a[230:231], v248 offset:8704
v_mfma_f32_32x32x16_bf16 v[32:47], v[116:119], a[48:51], v[32:47]
buffer_load_dwordx4 v[148:151], v246, s[12:15], s59 offen offset:0 sc1
s_waitcnt vmcnt(7)
v_mfma_f32_32x32x16_bf16 v[0:15], v[120:123], a[56:59], v[0:15]
ds_read_b64_tr_b16 a[232:233], v247 offset:10240
ds_read_b64_tr_b16 a[234:235], v247 offset:10752
v_mfma_f32_32x32x16_bf16 v[16:31], v[120:123], a[60:63], v[16:31]
buffer_load_dwordx4 v[152:155], v245, s[12:15], s59 offen offset:2048 sc1
s_waitcnt vmcnt(7)
v_mfma_f32_32x32x16_bf16 v[48:63], v[124:127], a[60:63], v[48:63]
ds_read_b64_tr_b16 a[236:237], v248 offset:10240
ds_read_b64_tr_b16 a[238:239], v248 offset:10752
v_mfma_f32_32x32x16_bf16 v[32:47], v[124:127], a[56:59], v[32:47]
buffer_load_dwordx4 v[156:159], v246, s[12:15], s59 offen offset:2048 sc1
s_waitcnt vmcnt(7)
v_mfma_f32_32x32x16_bf16 v[0:15], v[128:131], a[64:67], v[0:15]
v_mfma_f32_32x32x16_bf16 v[16:31], v[128:131], a[68:71], v[16:31]
s_add_u32 s59, s59, 4096
s_cmp_eq_u32 s59, s68
s_cselect_b32 s59, s66, s59
buffer_load_dwordx4 v[160:163], v245, s[12:15], s59 offen offset:0 sc1
s_waitcnt vmcnt(7)
v_mfma_f32_32x32x16_bf16 v[48:63], v[132:135], a[68:71], v[48:63]
v_mfma_f32_32x32x16_bf16 v[32:47], v[132:135], a[64:67], v[32:47]
buffer_load_dwordx4 v[164:167], v246, s[12:15], s59 offen offset:0 sc1
s_waitcnt vmcnt(7)
v_mfma_f32_32x32x16_bf16 v[0:15], v[136:139], a[72:75], v[0:15]
v_mfma_f32_32x32x16_bf16 v[16:31], v[136:139], a[76:79], v[16:31]
buffer_load_dwordx4 v[168:171], v245, s[12:15], s59 offen offset:2048 sc1
s_waitcnt vmcnt(7)
v_mfma_f32_32x32x16_bf16 v[48:63], v[140:143], a[76:79], v[48:63]
v_mfma_f32_32x32x16_bf16 v[32:47], v[140:143], a[72:75], v[32:47]
buffer_load_dwordx4 v[172:175], v246, s[12:15], s59 offen offset:2048 sc1
s_waitcnt vmcnt(7)
v_mfma_f32_32x32x16_bf16 v[0:15], v[144:147], a[80:83], v[0:15]
s_lshl_b32 s51, s57, 16
s_lshl_b32 s52, s69, 14
s_add_u32 s51, s51, s52
v_mfma_f32_32x32x16_bf16 v[16:31], v[144:147], a[84:87], v[16:31]
s_add_u32 s59, s59, 4096
s_cmp_eq_u32 s59, s68
s_cselect_b32 s59, s66, s59
buffer_load_dwordx4 v[176:179], v245, s[12:15], s59 offen offset:0 sc1
s_waitcnt vmcnt(7)
v_mfma_f32_32x32x16_bf16 v[48:63], v[148:151], a[84:87], v[48:63]
s_add_u32 s49, s51, 4718592
s_mov_b32 s64, s49
s_add_u32 s65, s49, 16384
v_mfma_f32_32x32x16_bf16 v[32:47], v[148:151], a[80:83], v[32:47]
buffer_load_dwordx4 v[180:183], v246, s[12:15], s59 offen offset:0 sc1
s_waitcnt vmcnt(7)
v_mfma_f32_32x32x16_bf16 v[0:15], v[152:155], a[88:91], v[0:15]
s_bfe_u32 s51, s45, 0x20008
s_mul_i32 s51, s51, 4096
s_add_u32 s49, s49, s51
v_mfma_f32_32x32x16_bf16 v[16:31], v[152:155], a[92:95], v[16:31]
buffer_load_dwordx4 v[184:187], v245, s[12:15], s59 offen offset:2048 sc1
s_waitcnt vmcnt(7)
v_mfma_f32_32x32x16_bf16 v[48:63], v[156:159], a[92:95], v[48:63]
s_bfe_u32 s51, s69, 0x10000
s_lshl_b32 s51, s51, 10
v_add_u32 v140, s51, v241
v_mfma_f32_32x32x16_bf16 v[32:47], v[156:159], a[88:91], v[32:47]
buffer_load_dwordx4 v[188:191], v246, s[12:15], s59 offen offset:2048 sc1
s_waitcnt vmcnt(7)
v_mfma_f32_32x32x16_bf16 v[0:15], v[160:163], a[96:99], v[0:15]
v_xor_b32 v141, 0x400, v140
s_xor_b32 s51, s69, 1
s_mul_i32 s51, s51, 12288
v_mfma_f32_32x32x16_bf16 v[16:31], v[160:163], a[100:103], v[16:31]
s_add_u32 s59, s59, 4096
s_cmp_eq_u32 s59, s68
s_cselect_b32 s59, s66, s59
buffer_load_dwordx4 v[192:195], v245, s[12:15], s59 offen offset:0 sc1
s_waitcnt vmcnt(7)
v_mfma_f32_32x32x16_bf16 v[48:63], v[164:167], a[100:103], v[48:63]
s_add_u32 s51, s51, 98304
v_add_u32 v137, s51, v241
s_xor_b32 s51, s69, 2
v_mfma_f32_32x32x16_bf16 v[32:47], v[164:167], a[96:99], v[32:47]
buffer_load_dwordx4 v[196:199], v246, s[12:15], s59 offen offset:0 sc1
s_waitcnt vmcnt(7)
v_mfma_f32_32x32x16_bf16 v[0:15], v[168:171], a[104:107], v[0:15]
s_mul_i32 s51, s51, 12288
s_add_u32 s51, s51, 102400
v_add_u32 v138, s51, v241
v_mfma_f32_32x32x16_bf16 v[16:31], v[168:171], a[108:111], v[16:31]
buffer_load_dwordx4 v[200:203], v245, s[12:15], s59 offen offset:2048 sc1
s_waitcnt vmcnt(7)
v_mfma_f32_32x32x16_bf16 v[48:63], v[172:175], a[108:111], v[48:63]
s_xor_b32 s51, s69, 3
s_mul_i32 s51, s51, 12288
s_add_u32 s51, s51, 106496
v_mfma_f32_32x32x16_bf16 v[32:47], v[172:175], a[104:107], v[32:47]
buffer_load_dwordx4 v[204:207], v246, s[12:15], s59 offen offset:2048 sc1
buffer_load_dwordx2 v[250:251], v249, s[12:15], 0 offen sc1
s_waitcnt vmcnt(8)
v_mfma_f32_32x32x16_bf16 v[0:15], v[176:179], a[112:115], v[0:15]
v_add_u32 v139, s51, v241
s_mul_i32 s51, s69, 12288
s_add_u32 s51, s51, 98304
v_mfma_f32_32x32x16_bf16 v[16:31], v[176:179], a[116:119], v[16:31]
s_add_u32 s59, s59, 4096
s_cmp_eq_u32 s59, s68
s_cselect_b32 s59, s66, s59
buffer_load_dwordx4 v[208:211], v245, s[12:15], s59 offen offset:0 sc1
s_waitcnt vmcnt(8)
v_mfma_f32_32x32x16_bf16 v[48:63], v[180:183], a[116:119], v[48:63]
v_add_u32 v136, s51, v241
v_sub_f32 v252, 1.0, v252
v_sub_f32 v253, 1.0, v253
v_mfma_f32_32x32x16_bf16 v[32:47], v[180:183], a[112:115], v[32:47]
buffer_load_dwordx4 v[212:215], v246, s[12:15], s59 offen offset:0 sc1
s_waitcnt vmcnt(8)
v_mfma_f32_32x32x16_bf16 v[0:15], v[184:187], a[120:123], v[0:15]
v_mfma_f32_32x32x16_bf16 v[16:31], v[184:187], a[124:127], v[16:31]
buffer_load_dwordx4 v[216:219], v245, s[12:15], s59 offen offset:2048 sc1
s_waitcnt vmcnt(8)
v_mfma_f32_32x32x16_bf16 v[48:63], v[188:191], a[124:127], v[48:63]
v_mfma_f32_32x32x16_bf16 v[32:47], v[188:191], a[120:123], v[32:47]
buffer_load_dwordx4 v[220:223], v246, s[12:15], s59 offen offset:2048 sc1
s_waitcnt vmcnt(8)
v_mfma_f32_32x32x16_bf16 v[0:15], v[192:195], a[128:131], v[0:15]
v_mfma_f32_32x32x16_bf16 v[16:31], v[192:195], a[132:135], v[16:31]
s_add_u32 s59, s59, 4096
s_cmp_eq_u32 s59, s68
s_cselect_b32 s59, s66, s59
buffer_load_dwordx4 v[224:227], v245, s[12:15], s59 offen offset:0 sc1
s_waitcnt vmcnt(8)
v_mfma_f32_32x32x16_bf16 v[48:63], v[196:199], a[132:135], v[48:63]
v_mfma_f32_32x32x16_bf16 v[32:47], v[196:199], a[128:131], v[32:47]
buffer_load_dwordx4 v[228:231], v246, s[12:15], s59 offen offset:0 sc1
s_waitcnt vmcnt(8)
v_mfma_f32_32x32x16_bf16 v[0:15], v[200:203], a[136:139], v[0:15]
v_mfma_f32_32x32x16_bf16 v[16:31], v[200:203], a[140:143], v[16:31]
buffer_load_dwordx4 v[232:235], v245, s[12:15], s59 offen offset:2048 sc1
s_waitcnt vmcnt(8)
v_mfma_f32_32x32x16_bf16 v[48:63], v[204:207], a[140:143], v[48:63]
v_mfma_f32_32x32x16_bf16 v[32:47], v[204:207], a[136:139], v[32:47]
buffer_load_dwordx4 v[236:239], v246, s[12:15], s59 offen offset:2048 sc1
s_waitcnt vmcnt(8)
v_cmp_ne_u64 vcc, s[46:47], v[250:251]
s_nop 1
s_cbranch_vccnz SLOW_22
s_waitcnt vmcnt(7)
v_mfma_f32_32x32x16_bf16 v[0:15], v[208:211], a[144:147], v[0:15]
v_mfma_f32_32x32x16_bf16 v[16:31], v[208:211], a[148:151], v[16:31]
s_mov_b32 s53, s49
buffer_load_dwordx4 v[68:71], v141, s[12:15], s49 offen offset:0 sc1
s_waitcnt vmcnt(7)
v_mfma_f32_32x32x16_bf16 v[48:63], v[212:215], a[148:151], v[48:63]
v_mfma_f32_32x32x16_bf16 v[32:47], v[212:215], a[144:147], v[32:47]
buffer_load_dwordx4 v[76:79], v141, s[12:15], s49 offen offset:2048 sc1
s_waitcnt vmcnt(7) lgkmcnt(15)
v_mfma_f32_32x32x16_bf16 v[0:15], v[216:219], a[152:155], v[0:15]
v_mfma_f32_32x32x16_bf16 v[16:31], v[216:219], a[156:159], v[16:31]
s_add_u32 s49, s49, 4096
s_cmp_eq_u32 s49, s65
s_cselect_b32 s49, s64, s49
buffer_load_dwordx4 v[84:87], v141, s[12:15], s49 offen offset:0 sc1
s_waitcnt vmcnt(7)
v_mfma_f32_32x32x16_bf16 v[48:63], v[220:223], a[156:159], v[48:63]
v_mfma_f32_32x32x16_bf16 v[32:47], v[220:223], a[152:155], v[32:47]
buffer_load_dwordx4 v[92:95], v141, s[12:15], s49 offen offset:2048 sc1
s_waitcnt vmcnt(7)
v_mfma_f32_32x32x16_bf16 v[0:15], v[224:227], a[160:163], v[0:15]
v_mfma_f32_32x32x16_bf16 v[16:31], v[224:227], a[164:167], v[16:31]
s_add_u32 s49, s49, 4096
s_cmp_eq_u32 s49, s65
s_cselect_b32 s49, s64, s49
buffer_load_dwordx4 v[100:103], v141, s[12:15], s49 offen offset:0 sc1
s_waitcnt vmcnt(7)
v_mfma_f32_32x32x16_bf16 v[48:63], v[228:231], a[164:167], v[48:63]
v_mfma_f32_32x32x16_bf16 v[32:47], v[228:231], a[160:163], v[32:47]
buffer_load_dwordx4 v[108:111], v141, s[12:15], s49 offen offset:2048 sc1
s_waitcnt vmcnt(7)
v_mfma_f32_32x32x16_bf16 v[0:15], v[232:235], a[168:171], v[0:15]
v_mfma_f32_32x32x16_bf16 v[16:31], v[232:235], a[172:175], v[16:31]
s_add_u32 s49, s49, 4096
s_cmp_eq_u32 s49, s65
s_cselect_b32 s49, s64, s49
buffer_load_dwordx4 v[116:119], v141, s[12:15], s49 offen offset:0 sc1
s_waitcnt vmcnt(7)
v_mfma_f32_32x32x16_bf16 v[48:63], v[236:239], a[172:175], v[48:63]
v_mfma_f32_32x32x16_bf16 v[32:47], v[236:239], a[168:171], v[32:47]
buffer_load_dwordx4 v[124:127], v141, s[12:15], s49 offen offset:2048 sc1
JOIN_23:
s_nop 2
v_pk_mul_f32 v[0:1], v[0:1], v[252:253] op_sel_hi:[1,0]
v_pk_mul_f32 v[2:3], v[2:3], v[252:253] op_sel_hi:[1,0]
v_pk_mul_f32 v[4:5], v[4:5], v[252:253] op_sel_hi:[1,0]
v_pk_mul_f32 v[6:7], v[6:7], v[252:253] op_sel_hi:[1,0]
v_pk_mul_f32 v[8:9], v[8:9], v[252:253] op_sel_hi:[1,0]
v_pk_mul_f32 v[10:11], v[10:11], v[252:253] op_sel_hi:[1,0]
v_pk_mul_f32 v[12:13], v[12:13], v[252:253] op_sel_hi:[1,0]
v_pk_mul_f32 v[14:15], v[14:15], v[252:253] op_sel_hi:[1,0]
v_pk_mul_f32 v[16:17], v[16:17], v[252:253] op_sel:[0,1] op_sel_hi:[1,1]
v_pk_mul_f32 v[18:19], v[18:19], v[252:253] op_sel:[0,1] op_sel_hi:[1,1]
v_pk_mul_f32 v[20:21], v[20:21], v[252:253] op_sel:[0,1] op_sel_hi:[1,1]
v_pk_mul_f32 v[22:23], v[22:23], v[252:253] op_sel:[0,1] op_sel_hi:[1,1]
v_pk_mul_f32 v[24:25], v[24:25], v[252:253] op_sel:[0,1] op_sel_hi:[1,1]
v_pk_mul_f32 v[26:27], v[26:27], v[252:253] op_sel:[0,1] op_sel_hi:[1,1]
v_pk_mul_f32 v[28:29], v[28:29], v[252:253] op_sel:[0,1] op_sel_hi:[1,1]
v_pk_mul_f32 v[30:31], v[30:31], v[252:253] op_sel:[0,1] op_sel_hi:[1,1]
v_pk_mul_f32 v[32:33], v[32:33], v[252:253] op_sel_hi:[1,0]
v_pk_mul_f32 v[34:35], v[34:35], v[252:253] op_sel_hi:[1,0]
v_pk_mul_f32 v[36:37], v[36:37], v[252:253] op_sel_hi:[1,0]
v_pk_mul_f32 v[38:39], v[38:39], v[252:253] op_sel_hi:[1,0]
v_pk_mul_f32 v[40:41], v[40:41], v[252:253] op_sel_hi:[1,0]
v_pk_mul_f32 v[42:43], v[42:43], v[252:253] op_sel_hi:[1,0]
v_pk_mul_f32 v[44:45], v[44:45], v[252:253] op_sel_hi:[1,0]
v_pk_mul_f32 v[46:47], v[46:47], v[252:253] op_sel_hi:[1,0]
v_pk_mul_f32 v[48:49], v[48:49], v[252:253] op_sel:[0,1] op_sel_hi:[1,1]
v_pk_mul_f32 v[50:51], v[50:51], v[252:253] op_sel:[0,1] op_sel_hi:[1,1]
v_pk_mul_f32 v[52:53], v[52:53], v[252:253] op_sel:[0,1] op_sel_hi:[1,1]
v_pk_mul_f32 v[54:55], v[54:55], v[252:253] op_sel:[0,1] op_sel_hi:[1,1]
v_pk_mul_f32 v[56:57], v[56:57], v[252:253] op_sel:[0,1] op_sel_hi:[1,1]
v_pk_mul_f32 v[58:59], v[58:59], v[252:253] op_sel:[0,1] op_sel_hi:[1,1]
v_pk_mul_f32 v[60:61], v[60:61], v[252:253] op_sel:[0,1] op_sel_hi:[1,1]
v_pk_mul_f32 v[62:63], v[62:63], v[252:253] op_sel:[0,1] op_sel_hi:[1,1]
s_waitcnt vmcnt(7)
s_nop 0
v_mfma_f32_32x32x16_bf16 v[48:63], a[180:183], v[68:71], v[48:63]
v_mfma_f32_32x32x16_bf16 v[16:31], a[176:179], v[68:71], v[16:31]
s_mov_b32 s49, s53
buffer_load_dwordx4 v[64:67], v140, s[12:15], s49 offen offset:0 sc1
s_waitcnt vmcnt(7)
v_mfma_f32_32x32x16_bf16 v[16:31], a[184:187], v[76:79], v[16:31]
v_mfma_f32_32x32x16_bf16 v[48:63], a[188:191], v[76:79], v[48:63]
buffer_load_dwordx4 v[72:75], v140, s[12:15], s49 offen offset:2048 sc1
s_waitcnt vmcnt(7)
v_mfma_f32_32x32x16_bf16 v[48:63], a[196:199], v[84:87], v[48:63]
v_mfma_f32_32x32x16_bf16 v[16:31], a[192:195], v[84:87], v[16:31]
s_add_u32 s49, s49, 4096
s_cmp_eq_u32 s49, s65
s_cselect_b32 s49, s64, s49
buffer_load_dwordx4 v[80:83], v140, s[12:15], s49 offen offset:0 sc1
s_waitcnt vmcnt(7)
v_mfma_f32_32x32x16_bf16 v[16:31], a[200:203], v[92:95], v[16:31]
v_mfma_f32_32x32x16_bf16 v[48:63], a[204:207], v[92:95], v[48:63]
buffer_load_dwordx4 v[88:91], v140, s[12:15], s49 offen offset:2048 sc1
s_waitcnt vmcnt(7) lgkmcnt(12)
v_mfma_f32_32x32x16_bf16 v[48:63], a[212:215], v[100:103], v[48:63]
v_mfma_f32_32x32x16_bf16 v[16:31], a[208:211], v[100:103], v[16:31]
s_add_u32 s49, s49, 4096
s_cmp_eq_u32 s49, s65
s_cselect_b32 s49, s64, s49
buffer_load_dwordx4 v[96:99], v140, s[12:15], s49 offen offset:0 sc1
s_waitcnt vmcnt(7) lgkmcnt(10)
v_mfma_f32_32x32x16_bf16 v[16:31], a[216:219], v[108:111], v[16:31]
s_waitcnt lgkmcnt(8)
v_mfma_f32_32x32x16_bf16 v[48:63], a[220:223], v[108:111], v[48:63]
buffer_load_dwordx4 v[104:107], v140, s[12:15], s49 offen offset:2048 sc1
s_waitcnt vmcnt(7) lgkmcnt(4)
v_mfma_f32_32x32x16_bf16 v[48:63], a[228:231], v[116:119], v[48:63]
v_mfma_f32_32x32x16_bf16 v[16:31], a[224:227], v[116:119], v[16:31]
s_add_u32 s49, s49, 4096
s_cmp_eq_u32 s49, s65
s_cselect_b32 s49, s64, s49
buffer_load_dwordx4 v[112:115], v140, s[12:15], s49 offen offset:0 sc1
s_waitcnt vmcnt(7) lgkmcnt(2)
v_mfma_f32_32x32x16_bf16 v[16:31], a[232:235], v[124:127], v[16:31]
s_waitcnt lgkmcnt(0)
v_mfma_f32_32x32x16_bf16 v[48:63], a[236:239], v[124:127], v[48:63]
buffer_load_dwordx4 v[120:123], v140, s[12:15], s49 offen offset:2048 sc1
s_waitcnt vmcnt(7)
v_mfma_f32_32x32x16_bf16 v[32:47], a[180:183], v[64:67], v[32:47]
s_nop 6
ds_write_b128 v137, v[16:19] offset:0
v_mfma_f32_32x32x16_bf16 v[0:15], a[176:179], v[64:67], v[0:15]
ds_write_b128 v137, v[20:23] offset:1024
s_waitcnt vmcnt(6)
v_mfma_f32_32x32x16_bf16 v[32:47], a[188:191], v[72:75], v[32:47]
ds_write_b128 v137, v[24:27] offset:2048
v_mfma_f32_32x32x16_bf16 v[0:15], a[184:187], v[72:75], v[0:15]
ds_write_b128 v137, v[28:31] offset:3072
s_waitcnt vmcnt(5)
v_mfma_f32_32x32x16_bf16 v[32:47], a[196:199], v[80:83], v[32:47]
ds_write_b128 v139, v[48:51] offset:0
v_mfma_f32_32x32x16_bf16 v[0:15], a[192:195], v[80:83], v[0:15]
ds_write_b128 v139, v[52:55] offset:1024
s_waitcnt vmcnt(4)
v_mfma_f32_32x32x16_bf16 v[32:47], a[204:207], v[88:91], v[32:47]
ds_write_b128 v139, v[56:59] offset:2048
v_mfma_f32_32x32x16_bf16 v[0:15], a[200:203], v[88:91], v[0:15]
ds_write_b128 v139, v[60:63] offset:3072
s_waitcnt vmcnt(3)
v_mfma_f32_32x32x16_bf16 v[32:47], a[212:215], v[96:99], v[32:47]
v_mfma_f32_32x32x16_bf16 v[0:15], a[208:211], v[96:99], v[0:15]
s_waitcnt vmcnt(2)
v_mfma_f32_32x32x16_bf16 v[32:47], a[220:223], v[104:107], v[32:47]
s_waitcnt vmcnt(1)
v_mfma_f32_32x32x16_bf16 v[32:47], a[228:231], v[112:115], v[32:47]
s_waitcnt vmcnt(0)
v_mfma_f32_32x32x16_bf16 v[32:47], a[236:239], v[120:123], v[32:47]
v_mfma_f32_32x32x16_bf16 v[0:15], a[216:219], v[104:107], v[0:15]
v_mfma_f32_32x32x16_bf16 v[0:15], a[224:227], v[112:115], v[0:15]
s_nop 9
ds_write_b128 v138, v[32:35] offset:0
v_mfma_f32_32x32x16_bf16 v[0:15], a[232:235], v[120:123], v[0:15]
ds_write_b128 v138, v[36:39] offset:1024
ds_write_b128 v138, v[40:43] offset:2048
ds_write_b128 v138, v[44:47] offset:3072
s_waitcnt lgkmcnt(0)
s_barrier
ds_read_b128 v[16:19], v136 offset:0
ds_read_b128 v[32:35], v136 offset:4096
ds_read_b128 v[48:51], v136 offset:8192
ds_read_b128 v[20:23], v136 offset:1024
ds_read_b128 v[36:39], v136 offset:5120
ds_read_b128 v[52:55], v136 offset:9216
ds_read_b128 v[24:27], v136 offset:2048
ds_read_b128 v[40:43], v136 offset:6144
ds_read_b128 v[56:59], v136 offset:10240
ds_read_b128 v[28:31], v136 offset:3072
ds_read_b128 v[44:47], v136 offset:7168
ds_read_b128 v[60:63], v136 offset:11264
s_lshr_b32 s51, s69, 1
s_lshl_b32 s51, s51, 6
s_lshl_b32 s52, s58, 7
s_add_u32 s51, s51, s52
v_lshrrev_b32 v255, 5, v240
v_lshlrev_b32 v255, 4, v255
v_lshl_add_u32 v255, v254, 10, v255
s_waitcnt lgkmcnt(11)
v_pk_add_f32 v[0:1], v[0:1], v[16:17]
v_pk_add_f32 v[2:3], v[2:3], v[18:19]
s_waitcnt lgkmcnt(9)
v_pk_add_f32 v[32:33], v[32:33], v[48:49]
v_pk_add_f32 v[34:35], v[34:35], v[50:51]
v_pk_add_f32 v[0:1], v[0:1], v[32:33]
v_pk_add_f32 v[2:3], v[2:3], v[34:35]
v_cvt_pk_bf16_f32 v72, v0, v1
v_cvt_pk_bf16_f32 v73, v2, v3
s_waitcnt lgkmcnt(8)
v_pk_add_f32 v[4:5], v[4:5], v[20:21]
v_pk_add_f32 v[6:7], v[6:7], v[22:23]
s_waitcnt lgkmcnt(6)
v_pk_add_f32 v[36:37], v[36:37], v[52:53]
v_pk_add_f32 v[38:39], v[38:39], v[54:55]
v_pk_add_f32 v[4:5], v[4:5], v[36:37]
v_pk_add_f32 v[6:7], v[6:7], v[38:39]
v_cvt_pk_bf16_f32 v74, v4, v5
v_cvt_pk_bf16_f32 v75, v6, v7
s_nop 0
v_permlane32_swap_b32 v72, v74
v_permlane32_swap_b32 v73, v75
buffer_store_dwordx4 v[72:75], v255, s[40:43], s51 offen offset:0
s_waitcnt lgkmcnt(5)
v_pk_add_f32 v[8:9], v[8:9], v[24:25]
v_pk_add_f32 v[10:11], v[10:11], v[26:27]
s_waitcnt lgkmcnt(3)
v_pk_add_f32 v[40:41], v[40:41], v[56:57]
v_pk_add_f32 v[42:43], v[42:43], v[58:59]
v_pk_add_f32 v[8:9], v[8:9], v[40:41]
v_pk_add_f32 v[10:11], v[10:11], v[42:43]
v_cvt_pk_bf16_f32 v76, v8, v9
v_cvt_pk_bf16_f32 v77, v10, v11
s_waitcnt lgkmcnt(2)
v_pk_add_f32 v[12:13], v[12:13], v[28:29]
v_pk_add_f32 v[14:15], v[14:15], v[30:31]
s_waitcnt lgkmcnt(0)
v_pk_add_f32 v[44:45], v[44:45], v[60:61]
v_pk_add_f32 v[46:47], v[46:47], v[62:63]
v_pk_add_f32 v[12:13], v[12:13], v[44:45]
v_pk_add_f32 v[14:15], v[14:15], v[46:47]
v_cvt_pk_bf16_f32 v78, v12, v13
v_cvt_pk_bf16_f32 v79, v14, v15
s_nop 0
v_permlane32_swap_b32 v76, v78
v_permlane32_swap_b32 v77, v79
buffer_store_dwordx4 v[76:79], v255, s[40:43], s51 offen offset:32
s_endpgm
SLOW_22:
buffer_load_dwordx2 v[250:251], v249, s[12:15], 0 offen sc1
s_waitcnt vmcnt(8)
v_mfma_f32_32x32x16_bf16 v[0:15], v[208:211], a[144:147], v[0:15]
v_mfma_f32_32x32x16_bf16 v[16:31], v[208:211], a[148:151], v[16:31]
s_waitcnt vmcnt(7)
v_mfma_f32_32x32x16_bf16 v[48:63], v[212:215], a[148:151], v[48:63]
v_mfma_f32_32x32x16_bf16 v[32:47], v[212:215], a[144:147], v[32:47]
s_waitcnt vmcnt(6) lgkmcnt(15)
v_mfma_f32_32x32x16_bf16 v[0:15], v[216:219], a[152:155], v[0:15]
v_mfma_f32_32x32x16_bf16 v[16:31], v[216:219], a[156:159], v[16:31]
s_waitcnt vmcnt(5)
v_mfma_f32_32x32x16_bf16 v[48:63], v[220:223], a[156:159], v[48:63]
v_mfma_f32_32x32x16_bf16 v[32:47], v[220:223], a[152:155], v[32:47]
s_waitcnt vmcnt(4)
v_mfma_f32_32x32x16_bf16 v[0:15], v[224:227], a[160:163], v[0:15]
v_mfma_f32_32x32x16_bf16 v[16:31], v[224:227], a[164:167], v[16:31]
s_waitcnt vmcnt(3)
v_mfma_f32_32x32x16_bf16 v[48:63], v[228:231], a[164:167], v[48:63]
v_mfma_f32_32x32x16_bf16 v[32:47], v[228:231], a[160:163], v[32:47]
s_waitcnt vmcnt(2)
v_mfma_f32_32x32x16_bf16 v[0:15], v[232:235], a[168:171], v[0:15]
v_mfma_f32_32x32x16_bf16 v[16:31], v[232:235], a[172:175], v[16:31]
s_waitcnt vmcnt(1)
v_mfma_f32_32x32x16_bf16 v[48:63], v[236:239], a[172:175], v[48:63]
v_mfma_f32_32x32x16_bf16 v[32:47], v[236:239], a[168:171], v[32:47]
s_waitcnt vmcnt(0)
v_cmp_ne_u64 vcc, s[46:47], v[250:251]
s_mov_b32 s54, 0
s_nop 0
s_cbranch_vccz POLLD_25
POLL_24:
buffer_load_dwordx2 v[250:251], v249, s[12:15], 0 offen sc1
s_waitcnt vmcnt(0)
v_cmp_ne_u64 vcc, s[46:47], v[250:251]
s_nop 1
s_cbranch_vccz POLLD_25
s_add_u32 s54, s54, 1
s_cmp_lt_u32 s54, 200000
s_sleep 1
s_cbranch_scc1 POLL_24
POLLD_25:
s_mov_b32 s53, s49
buffer_load_dwordx4 v[68:71], v141, s[12:15], s49 offen offset:0 sc1
buffer_load_dwordx4 v[76:79], v141, s[12:15], s49 offen offset:2048 sc1
s_add_u32 s49, s49, 4096
s_cmp_eq_u32 s49, s65
s_cselect_b32 s49, s64, s49
buffer_load_dwordx4 v[84:87], v141, s[12:15], s49 offen offset:0 sc1
buffer_load_dwordx4 v[92:95], v141, s[12:15], s49 offen offset:2048 sc1
s_add_u32 s49, s49, 4096
s_cmp_eq_u32 s49, s65
s_cselect_b32 s49, s64, s49
buffer_load_dwordx4 v[100:103], v141, s[12:15], s49 offen offset:0 sc1
buffer_load_dwordx4 v[108:111], v141, s[12:15], s49 offen offset:2048 sc1
s_add_u32 s49, s49, 4096
s_cmp_eq_u32 s49, s65
s_cselect_b32 s49, s64, s49
buffer_load_dwordx4 v[116:119], v141, s[12:15], s49 offen offset:0 sc1
buffer_load_dwordx4 v[124:127], v141, s[12:15], s49 offen offset:2048 sc1
s_branch JOIN_23
PCEND:
)ASM" :: "s"(ka), "s"(wg), "v"(tid) : "v1","v2","v3","v4","v5","v6","v7","v8","v9","v10","v11","v12","v13","v14","v15","v16","v17","v18","v19","v20","v21","v22","v23","v24","v25","v26","v27","v28","v29","v30","v31","v32","v33","v34","v35","v36","v37","v38","v39","v40","v41","v42","v43","v44","v45","v46","v47","v48","v49","v50","v51","v52","v53","v54","v55","v56","v57","v58","v59","v60","v61","v62","v63","v64","v65","v66","v67","v68","v69","v70","v71","v72","v73","v74","v75","v76","v77","v78","v79","v80","v81","v82","v83","v84","v85","v86","v87","v88","v89","v90","v91","v92","v93","v94","v95","v96","v97","v98","v99","v100","v101","v102","v103","v104","v105","v106","v107","v108","v109","v110","v111","v112","v113","v114","v115","v116","v117","v118","v119","v120","v121","v122","v123","v124","v125","v126","v127","v128","v129","v130","v131","v132","v133","v134","v135","v136","v137","v138","v139","v140","v141","v142","v143","v144","v145","v146","v147","v148","v149","v150","v151","v152","v153","v154","v155","v156","v157","v158","v159","v160","v161","v162","v163","v164","v165","v166","v167","v168","v169","v170","v171","v172","v173","v174","v175","v176","v177","v178","v179","v180","v181","v182","v183","v184","v185","v186","v187","v188","v189","v190","v191","v192","v193","v194","v195","v196","v197","v198","v199","v200","v201","v202","v203","v204","v205","v206","v207","v208","v209","v210","v211","v212","v213","v214","v215","v216","v217","v218","v219","v220","v221","v222","v223","v224","v225","v226","v227","v228","v229","v230","v231","v232","v233","v234","v235","v236","v237","v238","v239","v240","v241","v242","v243","v244","v245","v246","v247","v248","v249","v250","v251","v252","v253","v254","v255","a0","a1","a2","a3","a4","a5","a6","a7","a8","a9","a10","a11","a12","a13","a14","a15","a16","a17","a18","a19","a20","a21","a22","a23","a24","a25","a26","a27","a28","a29","a30","a31","a32","a33","a34","a35","a36","a37","a38","a39","a40","a41","a42","a43","a44","a45","a46","a47","a48","a49","a50","a51","a52","a53","a54","a55","a56","a57","a58","a59","a60","a61","a62","a63","a64","a65","a66","a67","a68","a69","a70","a71","a72","a73","a74","a75","a76","a77","a78","a79","a80","a81","a82","a83","a84","a85","a86","a87","a88","a89","a90","a91","a92","a93","a94","a95","a96","a97","a98","a99","a100","a101","a102","a103","a104","a105","a106","a107","a108","a109","a110","a111","a112","a113","a114","a115","a116","a117","a118","a119","a120","a121","a122","a123","a124","a125","a126","a127","a128","a129","a130","a131","a132","a133","a134","a135","a136","a137","a138","a139","a140","a141","a142","a143","a144","a145","a146","a147","a148","a149","a150","a151","a152","a153","a154","a155","a156","a157","a158","a159","a160","a161","a162","a163","a164","a165","a166","a167","a168","a169","a170","a171","a172","a173","a174","a175","a176","a177","a178","a179","a180","a181","a182","a183","a184","a185","a186","a187","a188","a189","a190","a191","a192","a193","a194","a195","a196","a197","a198","a199","a200","a201","a202","a203","a204","a205","a206","a207","a208","a209","a210","a211","a212","a213","a214","a215","a216","a217","a218","a219","a220","a221","a222","a223","a224","a225","a226","a227","a228","a229","a230","a231","a232","a233","a234","a235","a236","a237","a238","a239","a240","a241","a242","a243","a244","a245","a246","a247","a248","a249","a250","a251","a252","a253","a254","a255","s3","s4","s5","s6","s7","s8","s9","s10","s11","s12","s13","s14","s15","s16","s17","s18","s19","s20","s21","s22","s23","s24","s25","s26","s27","s28","s29","s30","s31","s32","s33","s34","s35","s36","s37","s38","s39","s40","s41","s42","s43","s44","s45","s46","s47","s48","s49","s50","s51","s52","s53","s54","s55","s56","s57","s58","s59","s60","s61","s62","s63","s64","s65","s66","s67","s68","s69","s70","s71","s72","s73","s74","s75","s76","s77","s78","s79","s80","s81","s82","s83","s84","s85","s86","s87","s88","s89","s90","s91","s92","s93","s94","s95","s96","s97","s98","s99","s100","s101","vcc","scc","m0","memory");
}


static const uint32_t kMap[512] = { 0x100,0x0,0x10180,0x2000,0x441102,0x68000,0x451182,0x6a000,0xa82104,0xd0000,0xa92184,0xd2000,0xee3106,0x138000,0xef3186,0x13a000,0x4120,0x4120,0x141a0,0x6120,0x445122,0x6c120,0x4551a2,0x6e120,0xa86124,0xd4120,0xa961a4,0xd6120,0xee7126,0x13c120,0xef71a6,0x13e120,0x8140,0x8250,0x181c0,0xa250,0x449142,0x70250,0x4591c2,0x72250,0xa8a144,0xd8250,0xa9a1c4,0xda250,0xeeb146,0x140250,0xefb1c6,0x142250,0xc160,0xc380,0x1c1e0,0xe380,0x44d162,0x74380,0x45d1e2,0x76380,0xa8e164,0xdc380,0xa9e1e4,0xde380,0xeef166,0x144380,0xeff1e6,0x146380,0x301,0x10104,0x10381,0x12104,0x641303,0x78104,0x651383,0x7a104,0xaa2305,0xe0104,0xab2385,0xe2104,0xee3307,0x148102,0xef3387,0x14a102,0x4321,0x14224,0x143a1,0x16224,0x645323,0x7c224,0x6553a3,0x7e224,0xaa6325,0xe4224,0xab63a5,0xe6224,0xee7327,0x14c222,0xef73a7,0x14e222,0x8341,0x18354,0x183c1,0x1a354,0x649343,0x80354,0x6593c3,0x82354,0xaaa345,0xe8354,0xaba3c5,0xea354,0xeeb347,0x150352,0xefb3c7,0x152352,0xc361,0x1c084,0x1c3e1,0x1e084,0x64d363,0x84084,0x65d3e3,0x86084,0xaae365,0xec084,0xabe3e5,0xee084,0xeef367,0x154082,0xeff3e7,0x156082,0x400,0x20200,0x10403,0x22200,0x661406,0x88200,0x671409,0x8a200,0xaa240c,0xf0200,0xab240f,0xf2200,0x1103508,0x158204,0x1113588,0x15a204,0x4420,0x24322,0x14423,0x26322,0x665426,0x8c322,0x675429,0x8e322,0xaa642c,0xf4322,0xab642f,0xf6322,0x1107528,0x15c324,0x11175a8,0x15e324,0x8440,0x28054,0x18443,0x2a054,0x669446,0x90054,0x679449,0x92054,0xaaa44c,0xf8054,0xaba44f,0xfa054,0x110b548,0x160054,0x111b5c8,0x162054,0xc460,0x2c186,0x1c463,0x2e186,0x66d466,0x94186,0x67d469,0x96186,0xaae46c,0xfc186,0xabe46f,0xfe186,0x110f568,0x164184,0x111f5e8,0x166184,0x200680,0x30308,0x210683,0x32308,0x661686,0x98308,0x671689,0x9a308,0xca268c,0x100308,0xcb268f,0x102308,0x1103709,0x168306,0x1113789,0x16a306,0x2046a0,0x3402a,0x2146a3,0x3602a,0x6656a6,0x9c02a,0x6756a9,0x9e02a,0xca66ac,0x10402a,0xcb66af,0x10602a,0x1107729,0x16c026,0x11177a9,0x16e026,0x2086c0,0x3815c,0x2186c3,0x3a15c,0x6696c6,0xa015c,0x6796c9,0xa215c,0xcaa6cc,0x10815c,0xcba6cf,0x10a15c,0x110b749,0x170156,0x111b7c9,0x172156,0x20c6e0,0x3c28e,0x21c6e3,0x3e28e,0x66d6e6,0xa428e,0x67d6e9,0xa628e,0xcae6ec,0x10c28e,0xcbe6ef,0x10e28e,0x110f769,0x174286,0x111f7e9,0x176286,0x220801,0x40000,0x230804,0x42000,0x661807,0xa8000,0x67180a,0xaa000,0xcc280d,0x110000,0xcd2810,0x112000,0x1103812,0x178000,0x1113814,0x17a000,0x224821,0x44122,0x234824,0x46122,0x665827,0xac122,0x67582a,0xae122,0xcc682d,0x114122,0xcd6830,0x116122,0x1107832,0x17c122,0x1117834,0x17e122,0x228841,0x48254,0x238844,0x4a254,0x669847,0xb0254,0x67984a,0xb2254,0xcca84d,0x118254,0xcda850,0x11a254,0x110b852,0x180254,0x111b854,0x182254,0x22c861,0x4c386,0x23c864,0x4e386,0x66d867,0xb4386,0x67d86a,0xb6386,0xcce86d,0x11c386,0xcde870,0x11e386,0x110f872,0x184386,0x111f874,0x186386,0x220a81,0x50108,0x230a84,0x52108,0x881a87,0xb8108,0x891a8a,0xba108,0xcc2a8d,0x120108,0xcd2a90,0x122108,0x1303a92,0x188108,0x1313a94,0x18a108,0x224aa1,0x5422a,0x234aa4,0x5622a,0x885aa7,0xbc22a,0x895aaa,0xbe22a,0xcc6aad,0x12422a,0xcd6ab0,0x12622a,0x1307ab2,0x18c22a,0x1317ab4,0x18e22a,0x228ac1,0x5835c,0x238ac4,0x5a35c,0x889ac7,0xc035c,0x899aca,0xc235c,0xccaacd,0x12835c,0xcdaad0,0x12a35c,0x130bad2,0x19035c,0x131bad4,0x19235c,0x22cae1,0x5c08e,0x23cae4,0x5e08e,0x88dae7,0xc408e,0x89daea,0xc608e,0xcceaed,0x12c08e,0xcdeaf0,0x12e08e,0x130faf2,0x19408e,0x131faf4,0x19608e,0x420c02,0x60200,0x430c05,0x62200,0x881c08,0xc8200,0x891c0b,0xca200,0xec2c0e,0x130200,0xed2c11,0x132200,0x1323c13,0x198200,0x3333c15,0x19a200,0x424c22,0x64322,0x434c25,0x66322,0x885c28,0xcc322,0x895c2b,0xce322,0xec6c2e,0x134322,0xed6c31,0x136322,0x1327c33,0x19c322,0x1337c35,0x19e322,0x428c42,0x1b8054,0x438c45,0x1c4054,0x889c48,0x1d0054,0x899c4b,0x1dc054,0xecac4e,0x1e8054,0xedac51,0x1f4054,0x132bc53,0x1a0054,0x133bc55,0x1a2054,0x42cc62,0x1ba186,0x43cc65,0x1c6186,0x88dc68,0x1d2186,0x89dc6b,0x1de186,0xecec6e,0x1ea186,0xedec71,0x1f6186,0x132fc73,0x1a4186,0x133fc75,0x1a6186,0x440e82,0x1bc308,0x450e85,0x1c8308,0x881e88,0x1d4308,0x891e8b,0x1e0308,0xee2e8e,0x1ec308,0xef2e91,0x1f8308,0x1323e93,0x1a8308,0x1333e95,0x1aa308,0x444ea2,0x1be02a,0x454ea5,0x1ca02a,0x885ea8,0x1d602a,0x895eab,0x1e202a,0xee6eae,0x1ee02a,0xef6eb1,0x1fa02a,0x1327eb3,0x1ac02a,0x1337eb5,0x1ae02a,0x448ec2,0x1c015c,0x458ec5,0x1cc15c,0x889ec8,0x1d815c,0x899ecb,0x1e415c,0xeeaece,0x1f015c,0xefaed1,0x1fc15c,0x132bed3,0x1b015c,0x133bed5,0x1b215c,0x44cee2,0x1c228e,0x45cee5,0x1ce28e,0x88dee8,0x1da28e,0x89deeb,0x1e628e,0xeeeeee,0x1f228e,0xefeef1,0x1fe28e,0x132fef3,0x1b428e,0x133fef5,0x1b628e };

extern "C" void kernel_launch(void* const* d_in, const int* in_sizes, int n_in, void* d_out, int out_size,
                              void* d_ws, size_t ws_size, hipStream_t stream) {
  Args a;
  const void* p[10] = {d_in[0], d_in[8], d_ws, d_in[4], d_in[1], d_in[6], d_in[2], d_in[5], d_in[7], d_out};
  const uint32_t sz[10] = {2048u*512u*2u, 512u*2u, 8388608u, 2048u*1376u*2u, 512u*1536u*2u, 1376u*2048u*2u, 512u*512u*2u, 1376u*2u, 2048u*2u, 2048u*512u*2u};
  for (int i = 0; i < 10; ++i) { a.b[i].p = p[i]; a.b[i].bytes = sz[i]; a.b[i].w3 = 0x00020000u; }
  for (int i = 0; i < 512; ++i) a.map[i] = kMap[i];
  (void)hipFuncSetAttribute((const void*)fused_kernel, hipFuncAttributeMaxDynamicSharedMemorySize, 160 * 1024);
  fused_kernel<<<256, 256, 160 * 1024, stream>>>(a);
}
```
